```python
import torch

def _softmax(x, axis=-1):
    return torch.softmax(x, dim=axis)
def _gelu(x):
    return torch.nn.functional.gelu(x, approximate="tanh")
def _silu(x):
    return torch.nn.functional.silu(x)
def _sigmoid(x):
    return torch.sigmoid(x)
def _layernorm(x, axis=-1, eps=1e-5):
    m = x.mean(dim=axis, keepdim=True)
    v = x.var(dim=axis, keepdim=True, unbiased=False)
    return (x - m) / torch.sqrt(v + eps)
def _scatter_add(tgt, idx, src, axis=0):
    return tgt.clone().index_add_(axis, idx, src)
def _segment_sum(src, seg, n):
    out = torch.zeros((n,) + src.shape[1:], dtype=src.dtype)
    return out.index_add_(0, seg, src)
def _pad(x, pw):
    flat = tuple(v for pair in reversed(pw) for v in pair)
    return torch.nn.functional.pad(x, flat)

kernel_name = 'standalone__paged_block_table_kv_radixattention__decode'

def setup_inputs():

    _w = (lambda a: (lambda t: t.to(torch.bfloat16) if t.is_floating_point() else t)(torch.as_tensor(a)))
    B, H, Hkv, Dh = 4, 32, 8, 128
    Lk = 16384
    bs = 16
    nb = max(2, Lk // bs)
    P = max(1, nb // 2)
    n_phys = P + B * (nb - P)
    shared = torch.arange(P, dtype=torch.int64)
    priv = P + torch.arange(B * (nb - P), dtype=torch.int64).reshape(B, nb - P)
    bt = torch.cat([torch.tile(shared[None, :], (B, 1)), priv], dim=1)
    ctx = nb * bs - (torch.arange(1, B + 1, dtype=torch.int64) * 2654435761) % max(1, (nb * bs - P * bs) // 2)
    return {
        "q": _w((lambda *s: torch.randn(*s, dtype=torch.bfloat16) * 0.8)(B, H, Dh)),
        "k_cache": _w((lambda *s: torch.randn(*s, dtype=torch.bfloat16) * 0.8)(n_phys, bs, Hkv, Dh)),
        "v_cache": _w((lambda *s: torch.randn(*s, dtype=torch.bfloat16) * 0.05)(n_phys, bs, Hkv, Dh)),
        "block_table": _w(bt.to(torch.int64)),
        "ctx_lens": _w(torch.as_tensor(ctx).to(torch.int64)),
    }

def reference(q, k_cache, v_cache, block_table, ctx_lens):
    B, H, Dh = q.shape
    n_phys, bs, Hkv, _ = k_cache.shape
    nb = block_table.shape[1]
    Lk = nb * bs
    rep = H // Hkv
    k = k_cache[block_table].reshape(B, Lk, Hkv, Dh)
    v = v_cache[block_table].reshape(B, Lk, Hkv, Dh)
    k = (lambda x, n, axis: torch.repeat_interleave(x, n, dim=axis))(k, rep, axis=2)
    v = (lambda x, n, axis: torch.repeat_interleave(x, n, dim=axis))(v, rep, axis=2)
    scores = torch.einsum("bhd,bkhd->bhk", q, k) * (Dh ** -0.5)
    pos = torch.arange(Lk)
    valid = pos[None, :] < ctx_lens[:, None]
    scores = torch.where(valid[:, None, :], scores, -1e9)
    p = _softmax(scores, axis=-1)
    return torch.einsum("bhk,bkhd->bhd", p, v)
```

```cpp
#include <hip/hip_runtime.h>
#include <hip/hip_bf16.h>
#include <cstdint>

namespace {

constexpr int B = 4, H = 32, HKV = 8, D = 128, BS = 16, NB = 1024;
constexpr int LK = NB * BS;
constexpr int NSPLIT = 32;
constexpr int NWAVE = 8, NTHR = NWAVE * 64;
constexpr int MAXI = (B * NB + NSPLIT - 1) / NSPLIT;
#define RING 1
#define KV_AUX 2
constexpr float C2 = 0.088388347648318440f * 1.4426950408889634f;
constexpr int BLK_BYTES = BS * HKV * D * 2;

using u32x4 = __attribute__((ext_vector_type(4))) unsigned;
using u32x2 = __attribute__((ext_vector_type(2))) unsigned;
using f32x4 = __attribute__((ext_vector_type(4))) float;
using bf16x8 = __attribute__((ext_vector_type(8))) __bf16;
using s16x4 = __attribute__((ext_vector_type(4))) short;

constexpr int RED_ROW = D + 4;
constexpr int LDS_RED = 0;
constexpr int LDS_ML = LDS_RED + NWAVE * 16 * RED_ROW;
constexpr int LDS_CNT = LDS_ML + NWAVE * 16 * 2;
constexpr int LDS_ITEM = LDS_CNT + NWAVE * 4;
constexpr int LDS_WORDS = LDS_ITEM + MAXI * 2;

__device__ __forceinline__ unsigned cvtpk(float lo, float hi) {
  using f32x2 = __attribute__((ext_vector_type(2))) float;
  using bf16x2 = __attribute__((ext_vector_type(2))) __bf16;
  return __builtin_bit_cast(unsigned, __builtin_convertvector(f32x2{lo, hi}, bf16x2));
}
__device__ __forceinline__ float xg_max(float x) {
  auto s = __builtin_amdgcn_permlane16_swap(__float_as_uint(x), __float_as_uint(x), false, false);
  x = fmaxf(__uint_as_float(s[0]), __uint_as_float(s[1]));
  auto t = __builtin_amdgcn_permlane32_swap(__float_as_uint(x), __float_as_uint(x), false, false);
  return fmaxf(__uint_as_float(t[0]), __uint_as_float(t[1]));
}
__device__ __forceinline__ float xg_sum(float x) {
  auto s = __builtin_amdgcn_permlane16_swap(__float_as_uint(x), __float_as_uint(x), false, false);
  x = __uint_as_float(s[0]) + __uint_as_float(s[1]);
  auto t = __builtin_amdgcn_permlane32_swap(__float_as_uint(x), __float_as_uint(x), false, false);
  return __uint_as_float(t[0]) + __uint_as_float(t[1]);
}

struct Slot { u32x4 k[4]; u32x4 v[4]; };

__global__ void __launch_bounds__(NTHR, 2)
attn_split(const unsigned short* __restrict__ q, const unsigned short* __restrict__ kc, const unsigned short* __restrict__ vc,
           const int* __restrict__ bt, const int* __restrict__ ctx, float* __restrict__ pml, float* __restrict__ po, unsigned kv_bytes) {
  __shared__ __attribute__((aligned(16))) float lds[LDS_WORDS];
  int* ldsi = reinterpret_cast<int*>(lds);
  const int tid = threadIdx.x, lane = tid & 63, wave = __builtin_amdgcn_readfirstlane(tid >> 6);
  const int g = lane >> 4, c = lane & 15;
  const int h = blockIdx.x & 7, split = blockIdx.x >> 3;

  u32x2 tb[B];
#pragma unroll
  for (int b = 0; b < B; ++b) tb[b] = *reinterpret_cast<const u32x2*>(bt + b * NB + 2 * tid);
  int len[B]; bool zq[B];
#pragma unroll
  for (int b = 0; b < B; ++b) { int l = ctx[b]; zq[b] = l <= 0; len[b] = (l <= 0 || l > LK) ? LK : l; }
  u32x4 qf[4];
  {
    const int qb = c >> 2;
    const unsigned short* qrow = q + ((qb * H) + 4 * h + (c & 3)) * D + 8 * g;
#pragma unroll
    for (int s = 0; s < 4; ++s) qf[s] = *reinterpret_cast<const u32x4*>(qrow + 32 * s);
    const bool z = qb == 0 ? zq[0] : qb == 1 ? zq[1] : qb == 2 ? zq[2] : zq[3];
    if (z) {
#pragma unroll
      for (int s = 0; s < 4; ++s) qf[s] = u32x4{0, 0, 0, 0};
    }
  }
  unsigned cnts[2][B]; bool lead[2][B];
#pragma unroll
  for (int e = 0; e < 2; ++e) {
    const int j = 2 * tid + e;
    int nv[B];
#pragma unroll
    for (int b = 0; b < B; ++b) nv[b] = min(max(len[b] - BS * j, 0), BS);
#pragma unroll
    for (int b = 0; b < B; ++b) {
      bool ld = nv[b] > 0; unsigned cn = 0;
#pragma unroll
      for (int b2 = 0; b2 < B; ++b2) {
        const bool same = tb[b2][e] == tb[b][e] && nv[b2] > 0;
        if (b2 < b) ld = ld && !same;
        else cn |= same ? (unsigned)nv[b2] << (8 * b2) : 0u;
      }
      lead[e][b] = ld; cnts[e][b] = cn;
    }
  }
  int pre[2][B];
#pragma unroll
  for (int b = 0; b < B; ++b) {
    const unsigned long long m0 = __ballot(lead[0][b]), m1 = __ballot(lead[1][b]);
    const int p0 = __builtin_amdgcn_mbcnt_hi((unsigned)(m0 >> 32), __builtin_amdgcn_mbcnt_lo((unsigned)m0, 0));
    const int p1 = __builtin_amdgcn_mbcnt_hi((unsigned)(m1 >> 32), __builtin_amdgcn_mbcnt_lo((unsigned)m1, 0));
    pre[0][b] = p0 + p1; pre[1][b] = p0 + p1 + (lead[0][b] ? 1 : 0);
    if (lane == 0) ldsi[LDS_CNT + wave * 4 + b] = __popcll(m0) + __popcll(m1);
  }
  __syncthreads();
  int base[B]; int total = 0;
  {
    int cw[NWAVE][B];
#pragma unroll
    for (int w = 0; w < NWAVE; ++w) {
      const u32x4 t = *reinterpret_cast<const u32x4*>(ldsi + LDS_CNT + w * 4);
#pragma unroll
      for (int b = 0; b < B; ++b) cw[w][b] = __builtin_amdgcn_readfirstlane(t[b]);
    }
#pragma unroll
    for (int b = 0; b < B; ++b) {
      base[b] = total;
#pragma unroll
      for (int w = 0; w < NWAVE; ++w) { if (w < wave) base[b] += cw[w][b]; total += cw[w][b]; }
    }
  }
  const int lo = (total * split) / NSPLIT, hi = (total * (split + 1)) / NSPLIT;
#pragma unroll
  for (int e = 0; e < 2; ++e)
#pragma unroll
    for (int b = 0; b < B; ++b) {
      const int pos = base[b] + pre[e][b] - lo;
      if (lead[e][b] && pos >= 0 && pos < hi - lo)
        *reinterpret_cast<u32x2*>(ldsi + LDS_ITEM + 2 * pos) = u32x2{tb[b][e], cnts[e][b]};
    }
  __syncthreads();
  const int nit = hi - lo;
  const int n = nit > wave ? (nit - wave + NWAVE - 1) / NWAVE : 0;
  u32x2 item = u32x2{0, 0};
  if (lane < n) item = *reinterpret_cast<const u32x2*>(ldsi + LDS_ITEM + 2 * (wave + NWAVE * lane));

  const __amdgpu_buffer_rsrc_t krs = __builtin_amdgcn_make_buffer_rsrc((void*)kc, 0, kv_bytes, 0x00020000);
  const __amdgpu_buffer_rsrc_t vrs = __builtin_amdgcn_make_buffer_rsrc((void*)vc, 0, kv_bytes, 0x00020000);
  const int koff = c * 2048 + h * 256 + 16 * g;
  const int voff = g * 8192 + h * 256 + 16 * c;

  Slot ring[RING];
  auto issue = [&](Slot& sl, int i) {
    const unsigned phys = __builtin_amdgcn_readlane(item[0], i);
    const int so = phys * BLK_BYTES;
#pragma unroll
    for (int s = 0; s < 4; ++s) sl.k[s] = __builtin_amdgcn_raw_buffer_load_b128(krs, koff + 64 * s, so, KV_AUX);
#pragma unroll
    for (int j = 0; j < 4; ++j) sl.v[j] = __builtin_amdgcn_raw_buffer_load_b128(vrs, voff + 2048 * j, so, KV_AUX);
  };

  float m = -3.0e38f, l = 0.f;
  f32x4 acc[8];
#pragma unroll
  for (int t = 0; t < 8; ++t) acc[t] = f32x4{0.f, 0.f, 0.f, 0.f};
  const int csh = 8 * (c >> 2);

  auto compute = [&](const Slot& sl, int i) {
    const unsigned cn = __builtin_amdgcn_readlane(item[1], i);
    f32x4 sc = f32x4{0.f, 0.f, 0.f, 0.f};
#pragma unroll
    for (int s = 0; s < 4; ++s)
      sc = __builtin_amdgcn_mfma_f32_16x16x32_bf16(__builtin_bit_cast(bf16x8, sl.k[s]), __builtin_bit_cast(bf16x8, qf[s]), sc, 0, 0, 0);
    const int nv = (int)((cn >> csh) & 0xffu) - 4 * g;
    float x[4];
#pragma unroll
    for (int r = 0; r < 4; ++r) x[r] = r < nv ? sc[r] * C2 : -INFINITY;
    const float mx = xg_max(fmaxf(fmaxf(x[0], x[1]), fmaxf(x[2], x[3])));
    const float mn = fmaxf(m, mx);
    const float alpha = __builtin_amdgcn_exp2f(m - mn);
    m = mn;
    float p[4];
#pragma unroll
    for (int r = 0; r < 4; ++r) p[r] = __builtin_amdgcn_exp2f(x[r] - mn);
    l = l * alpha + ((p[0] + p[1]) + (p[2] + p[3]));
    const u32x2 pk = u32x2{cvtpk(p[0], p[1]), cvtpk(p[2], p[3])};
#pragma unroll
    for (int t = 0; t < 8; ++t) {
      const unsigned sel = (t & 1) ? 0x07060302u : 0x05040100u;
      const u32x2 vt = u32x2{__builtin_amdgcn_perm(sl.v[1][t >> 1], sl.v[0][t >> 1], sel),
                             __builtin_amdgcn_perm(sl.v[3][t >> 1], sl.v[2][t >> 1], sel)};
      acc[t] = __builtin_amdgcn_mfma_f32_16x16x16bf16_1k(__builtin_bit_cast(s16x4, vt), __builtin_bit_cast(s16x4, pk), acc[t] * alpha, 0, 0, 0);
    }
  };

#pragma unroll
  for (int u = 0; u < RING; ++u) if (u < n) issue(ring[u], u);
  for (int i = 0; i < n; i += RING) {
#pragma unroll
    for (int u = 0; u < RING; ++u) {
      if (i + u < n) {
        compute(ring[u], i + u);
        if (i + u + RING < n) issue(ring[u], i + u + RING);
      }
    }
  }

  l = xg_sum(l);
  {
    float* row = lds + LDS_RED + (wave * 16 + c) * RED_ROW + 32 * g;
#pragma unroll
    for (int r = 0; r < 4; ++r) {
      *reinterpret_cast<f32x4*>(row + 8 * r) = f32x4{acc[0][r], acc[1][r], acc[2][r], acc[3][r]};
      *reinterpret_cast<f32x4*>(row + 8 * r + 4) = f32x4{acc[4][r], acc[5][r], acc[6][r], acc[7][r]};
    }
    if (g == 0) { lds[LDS_ML + (wave * 16 + c) * 2] = m; lds[LDS_ML + (wave * 16 + c) * 2 + 1] = l; }
  }
  __syncthreads();
  {
    const int r = tid >> 5, d0 = (tid & 31) * 4;
    float M = -3.0e38f;
#pragma unroll
    for (int w = 0; w < NWAVE; ++w) M = fmaxf(M, lds[LDS_ML + (w * 16 + r) * 2]);
    float L = 0.f; f32x4 o = f32x4{0.f, 0.f, 0.f, 0.f};
#pragma unroll
    for (int w = 0; w < NWAVE; ++w) {
      const float f = __builtin_amdgcn_exp2f(lds[LDS_ML + (w * 16 + r) * 2] - M);
      L += f * lds[LDS_ML + (w * 16 + r) * 2 + 1];
      o += f * *reinterpret_cast<const f32x4*>(lds + LDS_RED + (w * 16 + r) * RED_ROW + d0);
    }
    const int orow = (r >> 2) * H + 4 * h + (r & 3);
    const size_t slot = (size_t)orow * NSPLIT + split;
    if (L > 0.f) *reinterpret_cast<f32x4*>(po + slot * D + d0) = o;
    if ((tid & 31) == 0) *reinterpret_cast<float2*>(pml + slot * 2) = make_float2(M, L);
  }
}

__global__ void __launch_bounds__(256)
attn_combine(const float* __restrict__ pml, const float* __restrict__ po, unsigned short* __restrict__ out) {
  __shared__ __attribute__((aligned(16))) float red[8][D];
  const int row = blockIdx.x, tid = threadIdx.x, lane = tid & 63;
  const int sg = tid >> 5, d0 = (tid & 31) * 4;
  const float2 ml = *reinterpret_cast<const float2*>(pml + ((size_t)row * NSPLIT + (lane & 31)) * 2);
  f32x4 a[4];
#pragma unroll
  for (int i = 0; i < 4; ++i) a[i] = *reinterpret_cast<const f32x4*>(po + ((size_t)row * NSPLIT + sg * 4 + i) * D + d0);
  float M = ml.x;
#pragma unroll
  for (int o = 16; o >= 1; o >>= 1) M = fmaxf(M, __shfl_xor(M, o));
  const float w = ml.y > 0.f ? __builtin_amdgcn_exp2f(ml.x - M) : 0.f;
  float L = w * ml.y;
#pragma unroll
  for (int o = 16; o >= 1; o >>= 1) L += __shfl_xor(L, o);
  f32x4 o4 = f32x4{0.f, 0.f, 0.f, 0.f};
#pragma unroll
  for (int i = 0; i < 4; ++i) {
    const float wi = __shfl(w, sg * 4 + i);
    const float li = __shfl(ml.y, sg * 4 + i);
    if (li > 0.f) o4 += wi * a[i];
  }
  *reinterpret_cast<f32x4*>(&red[sg][d0]) = o4;
  __syncthreads();
  if (tid < 64) {
    float x0 = 0.f, x1 = 0.f;
#pragma unroll
    for (int s = 0; s < 8; ++s) { x0 += red[s][2 * tid]; x1 += red[s][2 * tid + 1]; }
    const float inv = 1.f / L;
    reinterpret_cast<unsigned*>(out + (size_t)row * D)[tid] = cvtpk(x0 * inv, x1 * inv);
  }
}

}

extern "C" void kernel_launch(void* const* d_in, const int* in_sizes, int n_in, void* d_out, int out_size, void* d_ws, size_t ws_size,
                              hipStream_t stream) {
  const unsigned short* q = (const unsigned short*)d_in[0];
  const unsigned short* kc = (const unsigned short*)d_in[1];
  const unsigned short* vc = (const unsigned short*)d_in[2];
  const int* bt = (const int*)d_in[3];
  const int* ctx = (const int*)d_in[4];
  float* pml = (float*)d_ws;
  float* po = (float*)((char*)d_ws + 65536);
  const unsigned kv_bytes = (unsigned)in_sizes[1] * 2u;
  attn_split<<<HKV * NSPLIT, NTHR, 0, stream>>>(q, kc, vc, bt, ctx, pml, po, kv_bytes);
  attn_combine<<<B * H, 256, 0, stream>>>(pml, po, (unsigned short*)d_out);
}
```

```cpp
#include <hip/hip_runtime.h>
#include <hip/hip_bf16.h>
#include <cstdint>

namespace {

constexpr int B = 4, H = 32, HKV = 8, D = 128, BS = 16, NB = 1024;
constexpr int LK = NB * BS;
constexpr int NSPLIT = 32;
constexpr int NWAVE = 4, NTHR = NWAVE * 64;
constexpr int MAXI = (B * NB + NSPLIT - 1) / NSPLIT;
#define KV_AUX 2
constexpr float C2 = 0.088388347648318440f * 1.4426950408889634f;
constexpr int BLK_BYTES = BS * HKV * D * 2;
constexpr float NEG_BIG = -3.0e38f;

using u32x4 = __attribute__((ext_vector_type(4))) unsigned;
using u32x2 = __attribute__((ext_vector_type(2))) unsigned;
using f32x4 = __attribute__((ext_vector_type(4))) float;
using f32x2 = __attribute__((ext_vector_type(2))) float;
using bf16x8 = __attribute__((ext_vector_type(8))) __bf16;
using bf16x2 = __attribute__((ext_vector_type(2))) __bf16;
using s16x4 = __attribute__((ext_vector_type(4))) short;
typedef __attribute__((address_space(1))) unsigned long long gu64;
typedef __attribute__((address_space(1))) unsigned gu32;
#define RLX_AGENT __ATOMIC_RELAXED, __HIP_MEMORY_SCOPE_AGENT

constexpr size_t WS_EPOCH = 0;
constexpr size_t WS_EARLY = 4096;
constexpr size_t WS_ML = 8192;
constexpr size_t WS_PO = WS_ML + (size_t)B * H * NSPLIT * 16;
constexpr size_t WS_DBG = WS_PO + (size_t)B * H * NSPLIT * D * 8;

constexpr int RED_ROW = D + 4;
constexpr int LDS_K = 0;
constexpr int LDS_RED = LDS_K + NWAVE * 1024;
constexpr int LDS_ML = LDS_RED + NWAVE * 16 * RED_ROW;
constexpr int LDS_CNT = LDS_ML + NWAVE * 16 * 2;
constexpr int LDS_ITEM = LDS_CNT + NWAVE * 4;
constexpr int LDS_WORDS = LDS_ITEM + MAXI * 2;

__device__ __forceinline__ unsigned cvtpk(float lo, float hi) {
  return __builtin_bit_cast(unsigned, __builtin_convertvector(f32x2{lo, hi}, bf16x2));
}
__device__ __forceinline__ float xg_max(float x) {
  auto s = __builtin_amdgcn_permlane16_swap(__float_as_uint(x), __float_as_uint(x), false, false);
  x = fmaxf(__uint_as_float(s[0]), __uint_as_float(s[1]));
  auto t = __builtin_amdgcn_permlane32_swap(__float_as_uint(x), __float_as_uint(x), false, false);
  return fmaxf(__uint_as_float(t[0]), __uint_as_float(t[1]));
}
__device__ __forceinline__ float xg_sum(float x) {
  auto s = __builtin_amdgcn_permlane16_swap(__float_as_uint(x), __float_as_uint(x), false, false);
  x = __uint_as_float(s[0]) + __uint_as_float(s[1]);
  auto t = __builtin_amdgcn_permlane32_swap(__float_as_uint(x), __float_as_uint(x), false, false);
  return __uint_as_float(t[0]) + __uint_as_float(t[1]);
}
template <int CTRL> __device__ __forceinline__ float dppf(float x) {
  return __builtin_bit_cast(float, __builtin_amdgcn_mov_dpp(__builtin_bit_cast(int, x), CTRL, 0xf, 0xf, true));
}
constexpr int XOR1 = 0xB1, XOR2 = 0x4E, ROR4 = 0x124, ROR8 = 0x128;
__device__ __forceinline__ float wave_max(float x) {
  x = fmaxf(x, dppf<XOR1>(x)); x = fmaxf(x, dppf<XOR2>(x)); x = fmaxf(x, dppf<ROR4>(x)); x = fmaxf(x, dppf<ROR8>(x));
  return xg_max(x);
}
__device__ __forceinline__ float wave_sum(float x) {
  x += dppf<XOR1>(x); x += dppf<XOR2>(x); x += dppf<ROR4>(x); x += dppf<ROR8>(x);
  return xg_sum(x);
}
__device__ __forceinline__ void st_gran(gu64* p, unsigned tag, float v) {
  __hip_atomic_store(p, ((unsigned long long)tag << 32) | __float_as_uint(v), RLX_AGENT);
}
#define STAMP(i) do {} while (0)

__global__ void __launch_bounds__(NTHR, 2)
attn_decode(const unsigned short* __restrict__ q, const unsigned short* __restrict__ kc, const unsigned short* __restrict__ vc,
            const int* __restrict__ bt, const int* __restrict__ ctx, unsigned short* __restrict__ out, unsigned char* ws, unsigned long long kv_bytes64) {
  __shared__ __attribute__((aligned(16))) float lds[LDS_WORDS];
  int* ldsi = reinterpret_cast<int*>(lds);
  const int tid = threadIdx.x, lane = tid & 63, wave = __builtin_amdgcn_readfirstlane(tid >> 6);
  const int g = lane >> 4, c = lane & 15;
  const int h = blockIdx.x & 7, split = blockIdx.x >> 3;
  const unsigned kv_bytes = (unsigned)kv_bytes64;
  STAMP(0);

  u32x4 tb[B];
#pragma unroll
  for (int b = 0; b < B; ++b) tb[b] = *reinterpret_cast<const u32x4*>(bt + b * NB + 4 * tid);
  gu32* epoch_p = (gu32*)(ws + WS_EPOCH + h * 256);
  const unsigned epoch = __hip_atomic_load(epoch_p, RLX_AGENT);
  u32x4 qf[4];
  const int myb = c >> 2;
  {
    const unsigned short* qrow = q + (myb * H + 4 * h + (c & 3)) * D + 8 * g;
#pragma unroll
    for (int s = 0; s < 4; ++s) qf[s] = *reinterpret_cast<const u32x4*>(qrow + 32 * s);
  }
  int len[B]; unsigned zmask = 0;
#pragma unroll
  for (int b = 0; b < B; ++b) { const int l = ctx[b]; zmask |= (l <= 0 ? 1u : 0u) << b; len[b] = (l <= 0 || l > LK) ? LK : l; }

  unsigned cnts[4][B]; int pre[B];
  {
    int wtot[B];
#pragma unroll
    for (int b = 0; b < B; ++b) { pre[b] = 0; wtot[b] = 0; }
#pragma unroll
    for (int e = 0; e < 4; ++e) {
      unsigned nv[B];
#pragma unroll
      for (int b = 0; b < B; ++b) nv[b] = (unsigned)min(max(len[b] - BS * (4 * tid + e), 0), BS) << (8 * b);
#pragma unroll
      for (int b = 0; b < B; ++b) {
        unsigned cn = nv[b];
#pragma unroll
        for (int b2 = b + 1; b2 < B; ++b2) {
          const bool same = tb[b2][e] == tb[b][e] && nv[b] != 0;
          cn |= same ? nv[b2] : 0u;
          nv[b2] = same ? 0u : nv[b2];
        }
        cnts[e][b] = cn;
        const unsigned long long mk = __ballot(cn != 0);
        pre[b] = __builtin_amdgcn_mbcnt_hi((unsigned)(mk >> 32), __builtin_amdgcn_mbcnt_lo((unsigned)mk, pre[b]));
        wtot[b] += __popcll(mk);
      }
    }
    if (lane == 0) *reinterpret_cast<u32x4*>(ldsi + LDS_CNT + wave * 4) = u32x4{(unsigned)wtot[0], (unsigned)wtot[1], (unsigned)wtot[2], (unsigned)wtot[3]};
  }
  __syncthreads();
  int total = 0;
  {
    u32x4 cw[NWAVE];
#pragma unroll
    for (int w = 0; w < NWAVE; ++w) cw[w] = *reinterpret_cast<const u32x4*>(ldsi + LDS_CNT + w * 4);
#pragma unroll
    for (int b = 0; b < B; ++b) {
      pre[b] += total;
#pragma unroll
      for (int w = 0; w < NWAVE; ++w) { pre[b] += w < wave ? cw[w][b] : 0; total += cw[w][b]; }
    }
  }
  total = __builtin_amdgcn_readfirstlane(total);
  const int lo = (total * split) / NSPLIT, nit = (total * (split + 1)) / NSPLIT - lo;
#pragma unroll
  for (int b = 0; b < B; ++b) {
    unsigned pos = pre[b] - lo;
#pragma unroll
    for (int e = 0; e < 4; ++e) {
      if (cnts[e][b] != 0) {
        if (pos < (unsigned)nit) *reinterpret_cast<u32x2*>(ldsi + LDS_ITEM + 2 * pos) = u32x2{tb[b][e], cnts[e][b]};
        ++pos;
      }
    }
  }
  __syncthreads();
  const int n = (nit - wave + NWAVE - 1) / NWAVE;
  u32x2 item = u32x2{0, 0};
  if (lane < n) item = *reinterpret_cast<const u32x2*>(ldsi + LDS_ITEM + 2 * (wave + NWAVE * lane));

  const int goff = g * 8192 + h * 256 + 16 * c;
  u32x4 kr[4], vr[4];
  auto issue = [&](u32x4* r, const unsigned short* base, int i) {
    const __amdgpu_buffer_rsrc_t rs = __builtin_amdgcn_make_buffer_rsrc((void*)base, 0, i < n ? kv_bytes : 0u, 0x00020000);
    const unsigned so = __builtin_amdgcn_readlane(item[0], i) * BLK_BYTES;
#pragma unroll
    for (int j = 0; j < 4; ++j) r[j] = __builtin_amdgcn_raw_buffer_load_b128(rs, goff + 2048 * j, so, KV_AUX);
  };
  issue(kr, kc, 0);
  issue(vr, vc, 0);
  __builtin_amdgcn_sched_barrier(0);
  STAMP(1);

  const unsigned tag = epoch + 1u;
  unsigned bmask;
  {
    unsigned o = 0;
    if (lane < nit) o = ldsi[LDS_ITEM + 2 * lane + 1];
    if (lane + 64 < nit) o |= ldsi[LDS_ITEM + 2 * (lane + 64) + 1];
    bmask = (__ballot((o & 0xffu) != 0) ? 1u : 0u) | (__ballot((o & 0xff00u) != 0) ? 2u : 0u) | (__ballot((o & 0xff0000u) != 0) ? 4u : 0u) | (__ballot((o >> 24) != 0) ? 8u : 0u);
    if (tid == 0) __hip_atomic_store((gu64*)(ws + WS_EARLY) + h * NSPLIT + split, ((unsigned long long)tag << 32) | bmask, RLX_AGENT);
  }
  if ((zmask >> myb) & 1u) {
#pragma unroll
    for (int s = 0; s < 4; ++s) qf[s] = u32x4{0, 0, 0, 0};
  }

  float m = NEG_BIG, l = 0.f;
  f32x4 acc[8];
#pragma unroll
  for (int t = 0; t < 8; ++t) acc[t] = f32x4{0.f, 0.f, 0.f, 0.f};
  const int csh = 8 * myb;
  u32x4* kst = reinterpret_cast<u32x4*>(ldsi + LDS_K + wave * 1024);

  for (int i = 0; i < n; ++i) {
#pragma unroll
    for (int j = 0; j < 4; ++j) kst[(4 * g + j) * 16 + (c ^ (4 * g + j))] = kr[j];
    __builtin_amdgcn_sched_barrier(0);
    issue(kr, kc, i + 1);
    __builtin_amdgcn_sched_barrier(0);
    u32x2 vt[8];
#pragma unroll
    for (int t = 0; t < 8; ++t) {
      const unsigned sel = (t & 1) ? 0x07060302u : 0x05040100u;
      vt[t] = u32x2{__builtin_amdgcn_perm(vr[1][t >> 1], vr[0][t >> 1], sel), __builtin_amdgcn_perm(vr[3][t >> 1], vr[2][t >> 1], sel)};
      asm volatile("" : "+v"(vt[t]));
    }
    __builtin_amdgcn_sched_barrier(0);
    issue(vr, vc, i + 1);
    __builtin_amdgcn_sched_barrier(0);
    const unsigned cn = __builtin_amdgcn_readlane(item[1], i);
    f32x4 sc = f32x4{0.f, 0.f, 0.f, 0.f};
#pragma unroll
    for (int s = 0; s < 4; ++s) {
      const u32x4 kf = kst[c * 16 + ((4 * s + g) ^ c)];
      sc = __builtin_amdgcn_mfma_f32_16x16x32_bf16(__builtin_bit_cast(bf16x8, kf), __builtin_bit_cast(bf16x8, qf[s]), sc, 0, 0, 0);
    }
    const int nv = (int)((cn >> csh) & 0xffu) - 4 * g;
    float x[4];
#pragma unroll
    for (int r = 0; r < 4; ++r) x[r] = r < nv ? sc[r] * C2 : -INFINITY;
    const float mn = fmaxf(m, xg_max(fmaxf(fmaxf(x[0], x[1]), fmaxf(x[2], x[3]))));
    const float alpha = __builtin_amdgcn_exp2f(m - mn);
    m = mn;
    float p[4];
#pragma unroll
    for (int r = 0; r < 4; ++r) p[r] = __builtin_amdgcn_exp2f(x[r] - mn);
    l = l * alpha + ((p[0] + p[1]) + (p[2] + p[3]));
    const u32x2 pk = u32x2{cvtpk(p[0], p[1]), cvtpk(p[2], p[3])};
#pragma unroll
    for (int t = 0; t < 8; ++t)
      acc[t] = __builtin_amdgcn_mfma_f32_16x16x16bf16_1k(__builtin_bit_cast(s16x4, vt[t]), __builtin_bit_cast(s16x4, pk), acc[t] * alpha, 0, 0, 0);
  }
  STAMP(2);

  l = xg_sum(l);
  {
    float* row = lds + LDS_RED + (wave * 16 + c) * RED_ROW + 32 * g;
#pragma unroll
    for (int r = 0; r < 4; ++r) {
      *reinterpret_cast<f32x4*>(row + 8 * r) = f32x4{acc[0][r], acc[1][r], acc[2][r], acc[3][r]};
      *reinterpret_cast<f32x4*>(row + 8 * r + 4) = f32x4{acc[4][r], acc[5][r], acc[6][r], acc[7][r]};
    }
    if (g == 0) *reinterpret_cast<f32x2*>(lds + LDS_ML + (c * NWAVE + wave) * 2) = f32x2{m, l};
  }
  __syncthreads();
  STAMP(3);
  for (int b = 0; b < B; ++b) {
    if (!((bmask >> b) & 1u)) continue;
    const int r = 4 * b + g;
    const f32x4 ml0 = *reinterpret_cast<const f32x4*>(lds + LDS_ML + r * NWAVE * 2);
    const f32x4 ml1 = *reinterpret_cast<const f32x4*>(lds + LDS_ML + r * NWAVE * 2 + 4);
    const float M = fmaxf(fmaxf(ml0[0], ml0[2]), fmaxf(ml1[0], ml1[2]));
    const float f0 = __builtin_amdgcn_exp2f(ml0[0] - M), f1 = __builtin_amdgcn_exp2f(ml0[2] - M);
    const float f2 = __builtin_amdgcn_exp2f(ml1[0] - M), f3 = __builtin_amdgcn_exp2f(ml1[2] - M);
    const float L = f0 * ml0[1] + f1 * ml0[3] + f2 * ml1[1] + f3 * ml1[3];
    const float* rp = lds + LDS_RED + r * RED_ROW + 32 * wave + c;
    const float o0 = f0 * rp[0] + f1 * rp[16 * RED_ROW] + f2 * rp[32 * RED_ROW] + f3 * rp[48 * RED_ROW];
    const float o1 = f0 * rp[16] + f1 * rp[16 * RED_ROW + 16] + f2 * rp[32 * RED_ROW + 16] + f3 * rp[48 * RED_ROW + 16];
    const int slot = (b * H + 4 * h + g) * NSPLIT + split;
    gu64* po = (gu64*)(ws + WS_PO) + (size_t)slot * D + 32 * wave + c;
    st_gran(po, tag, o0);
    st_gran(po + 16, tag, o1);
    if (wave == 0 && c < 2) st_gran((gu64*)(ws + WS_ML) + slot * 2 + c, tag, c == 0 ? M : L);
  }
  STAMP(4);
  if (wave != 0) return;

  {
    const int r = split >> 1, b = r >> 2;
    const int orow = b * H + 4 * h + (r & 3);
    const unsigned long long t_start = __builtin_amdgcn_s_memrealtime();
    unsigned cmask;
    for (;;) {
      const unsigned long long e = __hip_atomic_load((gu64*)(ws + WS_EARLY) + h * NSPLIT + (lane & 31), RLX_AGENT);
      cmask = (unsigned)__ballot((e >> b) & 1u);
      if (__all((unsigned)(e >> 32) == tag)) break;
      if (__builtin_amdgcn_s_memrealtime() - t_start > 20000000ull) break;
      __builtin_amdgcn_s_sleep(2);
    }
    if (split == 0 && lane == 0) __hip_atomic_store(epoch_p, tag, RLX_AGENT);
    gu64* mlp = (gu64*)(ws + WS_ML) + orow * NSPLIT * 2 + lane;
    gu64* pop = (gu64*)(ws + WS_PO) + (size_t)orow * NSPLIT * D + 64 * (split & 1) + lane;
    float val[NSPLIT];
    unsigned pending = cmask;
    unsigned long long mlg;
    const bool mine = (cmask >> (lane >> 1)) & 1u;
    for (;;) {
      mlg = __hip_atomic_load(mlp, RLX_AGENT);
      unsigned long long x[NSPLIT];
#pragma unroll
      for (int k = 0; k < NSPLIT; ++k) if ((pending >> k) & 1u) x[k] = __hip_atomic_load(pop + k * D, RLX_AGENT);
#pragma unroll
      for (int k = 0; k < NSPLIT; ++k) if ((pending >> k) & 1u) {
        val[k] = __uint_as_float((unsigned)x[k]);
        if (__all((unsigned)(x[k] >> 32) == tag)) pending &= ~(1u << k);
      }
      if (pending == 0 && __all(!mine || (unsigned)(mlg >> 32) == tag)) break;
      if (__builtin_amdgcn_s_memrealtime() - t_start > 20000000ull) break;
      __builtin_amdgcn_s_sleep(1);
    }
    STAMP(5);
    const float mlv = __uint_as_float((unsigned)mlg);
    const float mk = mine ? dppf<0xA0>(mlv) : NEG_BIG;
    const float lk = mine ? dppf<0xF5>(mlv) : 0.f;
    const float M = wave_max(mk);
    const float wk = mine ? __builtin_amdgcn_exp2f(mk - M) : 0.f;
    const float L = 0.5f * wave_sum(wk * lk);
    float o = 0.f;
#pragma unroll
    for (int k = 0; k < NSPLIT; ++k) if ((cmask >> k) & 1u) o += __builtin_bit_cast(float, __builtin_amdgcn_readlane(__builtin_bit_cast(int, wk), 2 * k)) * val[k];
    o = o / L;
    out[orow * D + 64 * (split & 1) + lane] = (unsigned short)(cvtpk(o, o) & 0xffffu);
    STAMP(6);
  }
}

}

extern "C" void kernel_launch(void* const* d_in, const int* in_sizes, int n_in, void* d_out, int out_size, void* d_ws, size_t ws_size,
                              hipStream_t stream) {
  attn_decode<<<HKV * NSPLIT, NTHR, 0, stream>>>((const unsigned short*)d_in[0], (const unsigned short*)d_in[1], (const unsigned short*)d_in[2],
                                                 (const int*)d_in[3], (const int*)d_in[4], (unsigned short*)d_out, (unsigned char*)d_ws,
                                                 (unsigned long long)in_sizes[1] * 2ull);
}
```

```cpp
#define DIAG 0
#include <hip/hip_runtime.h>
#include <hip/hip_bf16.h>
#include <cstdint>

namespace {

constexpr int B = 4, H = 32, HKV = 8, D = 128, BS = 16, NB = 1024;
constexpr int LK = NB * BS;
constexpr int NSPLIT = 32;
constexpr int NWAVE = 4, NTHR = NWAVE * 64;
constexpr int MAXI = (B * NB + NSPLIT - 1) / NSPLIT;
#define KV_AUX 0
constexpr float C2 = 0.088388347648318440f * 1.4426950408889634f;
constexpr int BLK_BYTES = BS * HKV * D * 2;
constexpr float NEG_BIG = -3.0e38f;

using u32x4 = __attribute__((ext_vector_type(4))) unsigned;
using u32x2 = __attribute__((ext_vector_type(2))) unsigned;
using f32x4 = __attribute__((ext_vector_type(4))) float;
using f32x2 = __attribute__((ext_vector_type(2))) float;
using bf16x8 = __attribute__((ext_vector_type(8))) __bf16;
using bf16x2 = __attribute__((ext_vector_type(2))) __bf16;
using s16x4 = __attribute__((ext_vector_type(4))) short;
typedef __attribute__((address_space(1))) unsigned long long gu64;
typedef __attribute__((address_space(1))) unsigned gu32;
#define RLX_AGENT __ATOMIC_RELAXED, __HIP_MEMORY_SCOPE_AGENT

constexpr size_t WS_EPOCH = 0;
constexpr size_t WS_EARLY = 4096;
constexpr size_t WS_ML = 8192;
constexpr size_t WS_PO = WS_ML + (size_t)B * H * NSPLIT * 16;
constexpr size_t WS_DBG = WS_PO + (size_t)B * H * NSPLIT * D * 8;

constexpr int RED_ROW = D + 4;
constexpr int LDS_K = 0;
constexpr int LDS_RED = LDS_K + NWAVE * 1024;
constexpr int LDS_ML = LDS_RED + NWAVE * 16 * RED_ROW;
constexpr int LDS_CNT = LDS_ML + NWAVE * 16 * 2;
constexpr int LDS_ITEM = LDS_CNT + NWAVE * 4;
constexpr int LDS_WORDS = LDS_ITEM + MAXI * 2;

__device__ __forceinline__ unsigned cvtpk(float lo, float hi) {
  return __builtin_bit_cast(unsigned, __builtin_convertvector(f32x2{lo, hi}, bf16x2));
}
__device__ __forceinline__ float xg_max(float x) {
  auto s = __builtin_amdgcn_permlane16_swap(__float_as_uint(x), __float_as_uint(x), false, false);
  x = fmaxf(__uint_as_float(s[0]), __uint_as_float(s[1]));
  auto t = __builtin_amdgcn_permlane32_swap(__float_as_uint(x), __float_as_uint(x), false, false);
  return fmaxf(__uint_as_float(t[0]), __uint_as_float(t[1]));
}
__device__ __forceinline__ float xg_sum(float x) {
  auto s = __builtin_amdgcn_permlane16_swap(__float_as_uint(x), __float_as_uint(x), false, false);
  x = __uint_as_float(s[0]) + __uint_as_float(s[1]);
  auto t = __builtin_amdgcn_permlane32_swap(__float_as_uint(x), __float_as_uint(x), false, false);
  return __uint_as_float(t[0]) + __uint_as_float(t[1]);
}
template <int CTRL> __device__ __forceinline__ float dppf(float x) {
  return __builtin_bit_cast(float, __builtin_amdgcn_mov_dpp(__builtin_bit_cast(int, x), CTRL, 0xf, 0xf, true));
}
constexpr int XOR1 = 0xB1, XOR2 = 0x4E, ROR4 = 0x124, ROR8 = 0x128;
__device__ __forceinline__ float wave_max(float x) {
  x = fmaxf(x, dppf<XOR1>(x)); x = fmaxf(x, dppf<XOR2>(x)); x = fmaxf(x, dppf<ROR4>(x)); x = fmaxf(x, dppf<ROR8>(x));
  return xg_max(x);
}
__device__ __forceinline__ float wave_sum(float x) {
  x += dppf<XOR1>(x); x += dppf<XOR2>(x); x += dppf<ROR4>(x); x += dppf<ROR8>(x);
  return xg_sum(x);
}
__device__ __forceinline__ void st_gran(gu64* p, unsigned tag, float v) {
  __hip_atomic_store(p, ((unsigned long long)tag << 32) | __float_as_uint(v), RLX_AGENT);
}
#define STAMP(i) do { if ((i) == 0) diag_t0 = __builtin_amdgcn_s_memrealtime(); if ((i) == DIAG) diag_x = __builtin_amdgcn_s_memrealtime(); } while (0)

__global__ void __launch_bounds__(NTHR, 2)
attn_decode(const unsigned short* __restrict__ q, const unsigned short* __restrict__ kc, const unsigned short* __restrict__ vc,
            const int* __restrict__ bt, const int* __restrict__ ctx, unsigned short* __restrict__ out, unsigned char* ws, unsigned long long kv_bytes64) {
  __shared__ __attribute__((aligned(16))) float lds[LDS_WORDS];
  int* ldsi = reinterpret_cast<int*>(lds);
  const int tid = threadIdx.x, lane = tid & 63, wave = __builtin_amdgcn_readfirstlane(tid >> 6);
  const int g = lane >> 4, c = lane & 15;
  const int h = blockIdx.x & 7, split = blockIdx.x >> 3;
  const unsigned kv_bytes = (unsigned)kv_bytes64;
  unsigned long long diag_t0 = 0, diag_x = 0;
  STAMP(0);

  u32x4 tb[B];
#pragma unroll
  for (int b = 0; b < B; ++b) tb[b] = *reinterpret_cast<const u32x4*>(bt + b * NB + 4 * tid);
  gu32* epoch_p = (gu32*)(ws + WS_EPOCH + h * 256);
  const unsigned epoch = __hip_atomic_load(epoch_p, RLX_AGENT);
  u32x4 qf[4];
  const int myb = c >> 2;
  {
    const unsigned short* qrow = q + (myb * H + 4 * h + (c & 3)) * D + 8 * g;
#pragma unroll
    for (int s = 0; s < 4; ++s) qf[s] = *reinterpret_cast<const u32x4*>(qrow + 32 * s);
  }
  int len[B]; unsigned zmask = 0;
#pragma unroll
  for (int b = 0; b < B; ++b) { const int l = ctx[b]; zmask |= (l <= 0 ? 1u : 0u) << b; len[b] = (l <= 0 || l > LK) ? LK : l; }

  unsigned cnts[4][B]; int pre[B];
  {
    int wtot[B];
#pragma unroll
    for (int b = 0; b < B; ++b) { pre[b] = 0; wtot[b] = 0; }
#pragma unroll
    for (int e = 0; e < 4; ++e) {
      unsigned nv[B];
#pragma unroll
      for (int b = 0; b < B; ++b) nv[b] = (unsigned)min(max(len[b] - BS * (4 * tid + e), 0), BS) << (8 * b);
#pragma unroll
      for (int b = 0; b < B; ++b) {
        unsigned cn = nv[b];
#pragma unroll
        for (int b2 = b + 1; b2 < B; ++b2) {
          const bool same = tb[b2][e] == tb[b][e] && nv[b] != 0;
          cn |= same ? nv[b2] : 0u;
          nv[b2] = same ? 0u : nv[b2];
        }
        cnts[e][b] = cn;
        const unsigned long long mk = __ballot(cn != 0);
        pre[b] = __builtin_amdgcn_mbcnt_hi((unsigned)(mk >> 32), __builtin_amdgcn_mbcnt_lo((unsigned)mk, pre[b]));
        wtot[b] += __popcll(mk);
      }
    }
    if (lane == 0) *reinterpret_cast<u32x4*>(ldsi + LDS_CNT + wave * 4) = u32x4{(unsigned)wtot[0], (unsigned)wtot[1], (unsigned)wtot[2], (unsigned)wtot[3]};
  }
  __syncthreads();
  int total = 0;
  {
    u32x4 cw[NWAVE];
#pragma unroll
    for (int w = 0; w < NWAVE; ++w) cw[w] = *reinterpret_cast<const u32x4*>(ldsi + LDS_CNT + w * 4);
#pragma unroll
    for (int b = 0; b < B; ++b) {
      pre[b] += total;
#pragma unroll
      for (int w = 0; w < NWAVE; ++w) { pre[b] += w < wave ? cw[w][b] : 0; total += cw[w][b]; }
    }
  }
  total = __builtin_amdgcn_readfirstlane(total);
  const int lo = (total * split) / NSPLIT, nit = (total * (split + 1)) / NSPLIT - lo;
#pragma unroll
  for (int b = 0; b < B; ++b) {
    unsigned pos = pre[b] - lo;
#pragma unroll
    for (int e = 0; e < 4; ++e) {
      if (cnts[e][b] != 0) {
        if (pos < (unsigned)nit) *reinterpret_cast<u32x2*>(ldsi + LDS_ITEM + 2 * pos) = u32x2{tb[b][e], cnts[e][b]};
        ++pos;
      }
    }
  }
  __syncthreads();
  const int n = (nit - wave + NWAVE - 1) / NWAVE;
  u32x2 item = u32x2{0, 0};
  if (lane < n) item = *reinterpret_cast<const u32x2*>(ldsi + LDS_ITEM + 2 * (wave + NWAVE * lane));

  const int goff = g * 8192 + h * 256 + 16 * c;
  u32x4 kr[4], vr[4];
  auto issue = [&](u32x4* r, const unsigned short* base, int i) {
    const __amdgpu_buffer_rsrc_t rs = __builtin_amdgcn_make_buffer_rsrc((void*)base, 0, i < n ? kv_bytes : 0u, 0x00020000);
    const unsigned so = __builtin_amdgcn_readlane(item[0], i) * BLK_BYTES;
#pragma unroll
    for (int j = 0; j < 4; ++j) r[j] = __builtin_amdgcn_raw_buffer_load_b128(rs, goff + 2048 * j, so, KV_AUX);
  };
  issue(kr, kc, 0);
  issue(vr, vc, 0);
  __builtin_amdgcn_sched_barrier(0);
  STAMP(1);

  const unsigned tag = epoch + 1u;
  unsigned bmask;
  {
    unsigned o = 0;
    if (lane < nit) o = ldsi[LDS_ITEM + 2 * lane + 1];
    if (lane + 64 < nit) o |= ldsi[LDS_ITEM + 2 * (lane + 64) + 1];
    bmask = (__ballot((o & 0xffu) != 0) ? 1u : 0u) | (__ballot((o & 0xff00u) != 0) ? 2u : 0u) | (__ballot((o & 0xff0000u) != 0) ? 4u : 0u) | (__ballot((o >> 24) != 0) ? 8u : 0u);
    if (tid == 0) __hip_atomic_store((gu64*)(ws + WS_EARLY) + h * NSPLIT + split, ((unsigned long long)tag << 32) | bmask, RLX_AGENT);
  }
  if ((zmask >> myb) & 1u) {
#pragma unroll
    for (int s = 0; s < 4; ++s) qf[s] = u32x4{0, 0, 0, 0};
  }

  float m = NEG_BIG, l = 0.f;
  f32x4 acc[8];
#pragma unroll
  for (int t = 0; t < 8; ++t) acc[t] = f32x4{0.f, 0.f, 0.f, 0.f};
  const int csh = 8 * myb;
  u32x4* kst = reinterpret_cast<u32x4*>(ldsi + LDS_K + wave * 1024);

  for (int i = 0; i < n; ++i) {
#pragma unroll
    for (int j = 0; j < 4; ++j) kst[(4 * g + j) * 16 + (c ^ (4 * g + j))] = kr[j];
    __builtin_amdgcn_sched_barrier(0);
    if (i == 0) STAMP(7);
    issue(kr, kc, i + 1);
    __builtin_amdgcn_sched_barrier(0);
    u32x2 vt[8];
#pragma unroll
    for (int t = 0; t < 8; ++t) {
      const unsigned sel = (t & 1) ? 0x07060302u : 0x05040100u;
      vt[t] = u32x2{__builtin_amdgcn_perm(vr[1][t >> 1], vr[0][t >> 1], sel), __builtin_amdgcn_perm(vr[3][t >> 1], vr[2][t >> 1], sel)};
      asm volatile("" : "+v"(vt[t]));
    }
    __builtin_amdgcn_sched_barrier(0);
    issue(vr, vc, i + 1);
    __builtin_amdgcn_sched_barrier(0);
    const unsigned cn = __builtin_amdgcn_readlane(item[1], i);
    f32x4 sc = f32x4{0.f, 0.f, 0.f, 0.f};
#pragma unroll
    for (int s = 0; s < 4; ++s) {
      const u32x4 kf = kst[c * 16 + ((4 * s + g) ^ c)];
      sc = __builtin_amdgcn_mfma_f32_16x16x32_bf16(__builtin_bit_cast(bf16x8, kf), __builtin_bit_cast(bf16x8, qf[s]), sc, 0, 0, 0);
    }
    const int nv = (int)((cn >> csh) & 0xffu) - 4 * g;
    float x[4];
#pragma unroll
    for (int r = 0; r < 4; ++r) x[r] = r < nv ? sc[r] * C2 : -INFINITY;
    const float mn = fmaxf(m, xg_max(fmaxf(fmaxf(x[0], x[1]), fmaxf(x[2], x[3]))));
    const float alpha = __builtin_amdgcn_exp2f(m - mn);
    m = mn;
    float p[4];
#pragma unroll
    for (int r = 0; r < 4; ++r) p[r] = __builtin_amdgcn_exp2f(x[r] - mn);
    l = l * alpha + ((p[0] + p[1]) + (p[2] + p[3]));
    const u32x2 pk = u32x2{cvtpk(p[0], p[1]), cvtpk(p[2], p[3])};
#pragma unroll
    for (int t = 0; t < 8; ++t)
      acc[t] = __builtin_amdgcn_mfma_f32_16x16x16bf16_1k(__builtin_bit_cast(s16x4, vt[t]), __builtin_bit_cast(s16x4, pk), acc[t] * alpha, 0, 0, 0);
  }
  STAMP(2);

  l = xg_sum(l);
  {
    float* row = lds + LDS_RED + (wave * 16 + c) * RED_ROW + 32 * g;
#pragma unroll
    for (int r = 0; r < 4; ++r) {
      *reinterpret_cast<f32x4*>(row + 8 * r) = f32x4{acc[0][r], acc[1][r], acc[2][r], acc[3][r]};
      *reinterpret_cast<f32x4*>(row + 8 * r + 4) = f32x4{acc[4][r], acc[5][r], acc[6][r], acc[7][r]};
    }
    if (g == 0) *reinterpret_cast<f32x2*>(lds + LDS_ML + (c * NWAVE + wave) * 2) = f32x2{m, l};
  }
  __syncthreads();
  STAMP(3);
  for (int b = 0; b < B; ++b) {
    if (!((bmask >> b) & 1u)) continue;
    const int r = 4 * b + g;
    const f32x4 ml0 = *reinterpret_cast<const f32x4*>(lds + LDS_ML + r * NWAVE * 2);
    const f32x4 ml1 = *reinterpret_cast<const f32x4*>(lds + LDS_ML + r * NWAVE * 2 + 4);
    const float M = fmaxf(fmaxf(ml0[0], ml0[2]), fmaxf(ml1[0], ml1[2]));
    const float f0 = __builtin_amdgcn_exp2f(ml0[0] - M), f1 = __builtin_amdgcn_exp2f(ml0[2] - M);
    const float f2 = __builtin_amdgcn_exp2f(ml1[0] - M), f3 = __builtin_amdgcn_exp2f(ml1[2] - M);
    const float L = f0 * ml0[1] + f1 * ml0[3] + f2 * ml1[1] + f3 * ml1[3];
    const float* rp = lds + LDS_RED + r * RED_ROW + 32 * wave + c;
    const float o0 = f0 * rp[0] + f1 * rp[16 * RED_ROW] + f2 * rp[32 * RED_ROW] + f3 * rp[48 * RED_ROW];
    const float o1 = f0 * rp[16] + f1 * rp[16 * RED_ROW + 16] + f2 * rp[32 * RED_ROW + 16] + f3 * rp[48 * RED_ROW + 16];
    const int slot = (b * H + 4 * h + g) * NSPLIT + split;
    gu64* po = (gu64*)(ws + WS_PO) + (size_t)slot * D + 32 * wave + c;
    st_gran(po, tag, o0);
    st_gran(po + 16, tag, o1);
    if (wave == 0 && c < 2) st_gran((gu64*)(ws + WS_ML) + slot * 2 + c, tag, c == 0 ? M : L);
  }
  STAMP(4);
  if (wave != 0) return;

  {
    const int r = split >> 1, b = r >> 2;
    const int orow = b * H + 4 * h + (r & 3);
    const unsigned long long t_start = __builtin_amdgcn_s_memrealtime();
    unsigned cmask;
    for (;;) {
      const unsigned long long e = __hip_atomic_load((gu64*)(ws + WS_EARLY) + h * NSPLIT + (lane & 31), RLX_AGENT);
      cmask = (unsigned)__ballot((e >> b) & 1u);
      if (__all((unsigned)(e >> 32) == tag)) break;
      if (__builtin_amdgcn_s_memrealtime() - t_start > 20000000ull) break;
      __builtin_amdgcn_s_sleep(2);
    }
    if (split == 0 && lane == 0) __hip_atomic_store(epoch_p, tag, RLX_AGENT);
    gu64* mlp = (gu64*)(ws + WS_ML) + orow * NSPLIT * 2 + lane;
    gu64* pop = (gu64*)(ws + WS_PO) + (size_t)orow * NSPLIT * D + 64 * (split & 1) + lane;
    float val[NSPLIT];
    unsigned pending = cmask;
    unsigned long long mlg;
    const bool mine = (cmask >> (lane >> 1)) & 1u;
    for (;;) {
      mlg = __hip_atomic_load(mlp, RLX_AGENT);
      unsigned long long x[NSPLIT];
#pragma unroll
      for (int k = 0; k < NSPLIT; ++k) if ((pending >> k) & 1u) x[k] = __hip_atomic_load(pop + k * D, RLX_AGENT);
#pragma unroll
      for (int k = 0; k < NSPLIT; ++k) if ((pending >> k) & 1u) {
        val[k] = __uint_as_float((unsigned)x[k]);
        if (__all((unsigned)(x[k] >> 32) == tag)) pending &= ~(1u << k);
      }
      if (pending == 0 && __all(!mine || (unsigned)(mlg >> 32) == tag)) break;
      if (__builtin_amdgcn_s_memrealtime() - t_start > 20000000ull) break;
      __builtin_amdgcn_s_sleep(1);
    }
    STAMP(5);
    const float mlv = __uint_as_float((unsigned)mlg);
    const float mk = mine ? dppf<0xA0>(mlv) : NEG_BIG;
    const float lk = mine ? dppf<0xF5>(mlv) : 0.f;
    const float M = wave_max(mk);
    const float wk = mine ? __builtin_amdgcn_exp2f(mk - M) : 0.f;
    const float L = 0.5f * wave_sum(wk * lk);
    float o = 0.f;
#pragma unroll
    for (int k = 0; k < NSPLIT; ++k) if ((cmask >> k) & 1u) o += __builtin_bit_cast(float, __builtin_amdgcn_readlane(__builtin_bit_cast(int, wk), 2 * k)) * val[k];
    o = o / L;
    out[orow * D + 64 * (split & 1) + lane] = (unsigned short)(cvtpk(o, o) & 0xffffu);
    STAMP(6);
    while (__builtin_amdgcn_s_memrealtime() < diag_x + 6000ull) __builtin_amdgcn_s_sleep(1);
  }
}

}

extern "C" void kernel_launch(void* const* d_in, const int* in_sizes, int n_in, void* d_out, int out_size, void* d_ws, size_t ws_size,
                              hipStream_t stream) {
  attn_decode<<<HKV * NSPLIT, NTHR, 0, stream>>>((const unsigned short*)d_in[0], (const unsigned short*)d_in[1], (const unsigned short*)d_in[2],
                                                 (const int*)d_in[3], (const int*)d_in[4], (unsigned short*)d_out, (unsigned char*)d_ws,
                                                 (unsigned long long)in_sizes[1] * 2ull);
}
```

```cpp
#include <hip/hip_runtime.h>
#include <hip/hip_bf16.h>
#include <cstdint>

namespace {

constexpr int B = 4, H = 32, HKV = 8, D = 128, BS = 16, NB = 1024;
constexpr unsigned LK = NB * BS;
constexpr int NSPLIT = 32;
constexpr int NWAVE = 4, NTHR = NWAVE * 64;
#define KV_AUX 2
#define MAPMODE 0
#define ROT 1
#define POLL_MODE 2
#define POLL_SLEEP 1
#define WIN 6
#define CONS_SEL 0
#define SPIN_CAP (1u << 18)
constexpr float C2 = 0.088388347648318440f * 1.4426950408889634f;
constexpr int BLK_BYTES = BS * HKV * D * 2;
constexpr float NEG_BIG = -3.0e38f;
constexpr int AUX_SC1 = 16;
constexpr unsigned OOB = 0x80000000u;

using u32x4 = __attribute__((ext_vector_type(4))) unsigned;
using u32x2 = __attribute__((ext_vector_type(2))) unsigned;
using u64x2 = __attribute__((ext_vector_type(2))) unsigned long long;
using f32x4 = __attribute__((ext_vector_type(4))) float;
using f32x2 = __attribute__((ext_vector_type(2))) float;
using bf16x8 = __attribute__((ext_vector_type(8))) __bf16;
using bf16x2 = __attribute__((ext_vector_type(2))) __bf16;
using s16x4 = __attribute__((ext_vector_type(4))) short;

constexpr unsigned RB_LSE = 256, RB_PO = 512, RB_BYTES = RB_PO + NSPLIT * 512;
constexpr unsigned WS_USED = B * H * RB_BYTES;
constexpr size_t WS_DBG = WS_USED;

constexpr int RED_ROW = D + 4;
constexpr int LDS_LCP = 0;
constexpr int LDS_ML = 64;
constexpr int LDS_K = LDS_ML + NWAVE * 16 * 2;
constexpr int LDS_TAB = LDS_K + NWAVE * 1024;
constexpr int LDS_RED = LDS_TAB + B * NB;
constexpr int LDS_WORDS = LDS_RED + NWAVE * 16 * RED_ROW;

__device__ __forceinline__ unsigned cvtpk(float lo, float hi) {
  return __builtin_bit_cast(unsigned, __builtin_convertvector(f32x2{lo, hi}, bf16x2));
}
__device__ __forceinline__ float xg_max(float x) {
  auto s = __builtin_amdgcn_permlane16_swap(__float_as_uint(x), __float_as_uint(x), false, false);
  x = fmaxf(__uint_as_float(s[0]), __uint_as_float(s[1]));
  auto t = __builtin_amdgcn_permlane32_swap(__float_as_uint(x), __float_as_uint(x), false, false);
  return fmaxf(__uint_as_float(t[0]), __uint_as_float(t[1]));
}
__device__ __forceinline__ float xg_sum(float x) {
  auto s = __builtin_amdgcn_permlane16_swap(__float_as_uint(x), __float_as_uint(x), false, false);
  x = __uint_as_float(s[0]) + __uint_as_float(s[1]);
  auto t = __builtin_amdgcn_permlane32_swap(__float_as_uint(x), __float_as_uint(x), false, false);
  return __uint_as_float(t[0]) + __uint_as_float(t[1]);
}
template <int CTRL> __device__ __forceinline__ float dppf(float x) {
  return __builtin_bit_cast(float, __builtin_amdgcn_mov_dpp(__builtin_bit_cast(int, x), CTRL, 0xf, 0xf, true));
}
constexpr int XOR1 = 0xB1, XOR2 = 0x4E, ROR4 = 0x124, BCAST = 0x150;
template <int LANE>
__device__ __forceinline__ void first_diff(unsigned& cand, u32x4 a, u32x4 b) {
  const u64x2 x = __builtin_bit_cast(u64x2, a), y = __builtin_bit_cast(u64x2, b);
  const unsigned long long mk = __builtin_amdgcn_ballot_w64(x[0] != y[0]) | __builtin_amdgcn_ballot_w64(x[1] != y[1]);
  unsigned r; asm("s_ff1_i32_b64 %1, %2\n\tv_writelane_b32 %0, %1, %3" : "+v"(cand), "=&s"(r) : "s"(mk), "i"(LANE));
}
__device__ __forceinline__ unsigned clamp16(int x) { return (unsigned)min(max(x, 0), BS); }
#define STAMP(i) do {} while (0)
#define STAMP_FLUSH() do {} while (0)

__global__ void __attribute__((aligned(4096))) __launch_bounds__(NTHR, 2)
attn_decode(const int* __restrict__ bt, const int* __restrict__ ctx, const unsigned short* __restrict__ q, unsigned char* ws,
            const unsigned short* __restrict__ kc, const unsigned short* __restrict__ vc, unsigned short* __restrict__ out, unsigned long long kv_bytes64) {
  __shared__ __attribute__((aligned(16))) float lds[LDS_WORDS];
  unsigned* ldsu = reinterpret_cast<unsigned*>(lds);
  STAMP(0);
  const unsigned tid = threadIdx.x, lane = tid & 63, wave = __builtin_amdgcn_readfirstlane(tid >> 6);
  const unsigned g = lane >> 4, c = lane & 15;
  const unsigned split = blockIdx.x >> 3;
  const unsigned h = MAPMODE == 0 ? (blockIdx.x & 7) : ((blockIdx.x + split) & 7);
  const unsigned kv_bytes = (unsigned)kv_bytes64;

  ldsu[LDS_LCP + lane] = 0xffffffffu;
  asm volatile("s_waitcnt lgkmcnt(0)" :: "s"(bt), "s"(ctx), "s"(q), "s"(ws), "s"(kc), "s"(vc), "s"(out), "s"(kv_bytes) : "memory");
  const __amdgpu_buffer_rsrc_t btr = __builtin_amdgcn_make_buffer_rsrc((void*)bt, 0, B * NB * 4, 0x00020000);
  u32x4 tb[B];
#pragma unroll
  for (int b = 0; b < B; ++b) tb[b] = __builtin_amdgcn_raw_buffer_load_b128(btr, 16 * tid, b * NB * 4, 0);
  const u32x4 ctx4 = *reinterpret_cast<const u32x4*>(ctx);
  __builtin_amdgcn_sched_barrier(0);
  u32x4 qf[4];
  const unsigned myb = c >> 2;
  {
    const unsigned short* qrow = q + (myb * H + 4 * h + (c & 3)) * D + 8 * g;
#pragma unroll
    for (int s = 0; s < 4; ++s) qf[s] = *reinterpret_cast<const u32x4*>(qrow + 32 * s);
  }
  const __amdgpu_buffer_rsrc_t wsr = __builtin_amdgcn_make_buffer_rsrc((void*)ws, 0, WS_USED, 0x00020000);
  const unsigned crow = CONS_SEL == 0 ? split >> 1 : split & 15;
  const bool consumer = CONS_SEL == 0 ? (split & 1) != 0 : CONS_SEL == 1 ? split >= 16 : split < 16;
  const unsigned rb_pub = (h * 16 + 4 * wave + g) * RB_BYTES, rb_own = (h * 16 + crow) * RB_BYTES;
  const unsigned ep_pub = __builtin_amdgcn_raw_buffer_load_b32(wsr, rb_pub, 0, AUX_SC1);
  const unsigned ep_own = __builtin_amdgcn_raw_buffer_load_b32(wsr, 0, rb_own, AUX_SC1);
  unsigned goff = g * 8192 + h * 256 + 16 * c;
  unsigned goff2 = goff + 4096;
  asm volatile("s_barrier" : "+v"(goff), "+v"(goff2) :: "memory");
  __builtin_amdgcn_sched_barrier(0);
  STAMP(10);
  __builtin_amdgcn_sched_barrier(0);
#pragma unroll
  for (int b = 0; b < B; ++b) *reinterpret_cast<u32x4*>(ldsu + LDS_TAB + b * NB + 4 * tid) = tb[b];
  STAMP(11);
  {
    unsigned cand = 0xffffffffu;
    first_diff<0>(cand, tb[1], tb[0]);
    first_diff<1>(cand, tb[2], tb[0]);
    first_diff<2>(cand, tb[3], tb[0]);
    first_diff<3>(cand, tb[2], tb[1]);
    first_diff<4>(cand, tb[3], tb[1]);
    first_diff<5>(cand, tb[3], tb[2]);
    __hip_atomic_fetch_min(ldsu + LDS_LCP + lane, (cand << 2) | (wave << 8), __ATOMIC_RELAXED, __HIP_MEMORY_SCOPE_WORKGROUP);
  }
  __builtin_amdgcn_sched_barrier(0);
  const unsigned len0 = min(ctx4[0] - 1u, LK - 1u) + 1u, len1 = min(ctx4[1] - 1u, LK - 1u) + 1u;
  const unsigned len2 = min(ctx4[2] - 1u, LK - 1u) + 1u, len3 = min(ctx4[3] - 1u, LK - 1u) + 1u;
  const unsigned nc0 = (len0 + BS - 1) / BS, nc1 = (len1 + BS - 1) / BS, nc2 = (len2 + BS - 1) / BS, nc3 = (len3 + BS - 1) / BS;
  unsigned c10 = min(nc0, nc1), c20 = min(nc0, nc2), c30 = min(nc0, nc3), c21 = min(nc1, nc2), c31 = min(nc1, nc3), c32 = min(nc2, nc3);
  unsigned s01 = nc0 + nc1, s012 = s01 + nc2, s0123 = s012 + nc3;
  asm volatile("" : "+s"(c10), "+s"(c20), "+s"(c30), "+s"(c21), "+s"(c31), "+s"(c32), "+s"(s01), "+s"(s012), "+s"(s0123));
  asm volatile("s_waitcnt lgkmcnt(0)\n\ts_barrier" ::: "memory");
  STAMP(9);
  const u32x4 la = *reinterpret_cast<const u32x4*>(ldsu + LDS_LCP);
  const u32x2 lb = *reinterpret_cast<const u32x2*>(ldsu + LDS_LCP + 4);
  const unsigned l10 = min(la[0], c10), l20 = min(la[1], c20), l30 = min(la[2], c30), l21 = min(la[3], c21), l31 = min(lb[0], c31), l32 = min(lb[1], c32);
  const unsigned P1 = l10, P2 = max(l20, l21), P3 = max(l30, max(l31, l32));
  const unsigned B1 = nc0, B2 = s01 - P1, B3 = s012 - P1 - P2, total = s0123 - P1 - P2 - P3;
  const unsigned lo = (total * split) / NSPLIT, hi = (total * split + total) / NSPLIT;
  const unsigned nv_ = (hi - lo - wave + NWAVE - 1) >> 2;
  unsigned li = lane;
  if (ROT) { li += min(split >> 1, nv_); li = min(li, li - nv_); }
  const unsigned pos = lo + wave + NWAVE * li;
  unsigned idx = pos;
  idx = pos >= B1 ? pos + (NB + P1 - B1) : idx;
  idx = pos >= B2 ? pos + (2 * NB + P2 - B2) : idx;
  idx = pos >= B3 ? pos + (3 * NB + P3 - B3) : idx;
  idx &= B * NB - 1;
  const unsigned item0 = ldsu[LDS_TAB + idx];
  const int n = __builtin_amdgcn_readfirstlane(nv_);

#define GOFF(j) (((j) & 2 ? goff2 : goff) + 2048 * ((j) & 1))
  u32x4 kr[4], vr[4];
  auto rsrc = [&](const unsigned short* base, int i) { return __builtin_amdgcn_make_buffer_rsrc((void*)base, 0, i < n ? kv_bytes : 0u, 0x00020000); };
  {
    const unsigned so = __builtin_amdgcn_readfirstlane(item0) * BLK_BYTES;
#pragma unroll
    for (int j = 0; j < 4; ++j) kr[j] = __builtin_amdgcn_raw_buffer_load_b128(rsrc(kc, 0), GOFF(j), so, KV_AUX);
#pragma unroll
    for (int j = 0; j < WIN - 4; ++j) vr[j] = __builtin_amdgcn_raw_buffer_load_b128(rsrc(vc, 0), GOFF(j), so, KV_AUX);
  }
  __builtin_amdgcn_sched_barrier(0);
  STAMP(1);

  unsigned item1;
  {
    const unsigned ib = idx >> 10, j = idx & (NB - 1);
    const int t0 = BS * j;
    const unsigned x1 = ib == 0 ? l10 : 0u, x2 = ib == 0 ? l20 : ib == 1 ? l21 : 0u, x3 = ib == 0 ? l30 : ib == 1 ? l31 : l32;
    item1 = (ib == 0 ? clamp16(len0 - t0) : 0u) | ((ib == 1 || j < x1) ? clamp16(len1 - t0) << 8 : 0u) |
            ((ib == 2 || j < x2) ? clamp16(len2 - t0) << 16 : 0u) | ((ib == 3 || j < x3) ? clamp16(len3 - t0) << 24 : 0u);
  }
  unsigned cmask; bool publish;
  {
    const unsigned k = lane & 31, klo = (total * k) / NSPLIT, khi = (total * k + total) / NSPLIT;
    const unsigned s1 = max(klo, B1), s2 = max(klo, B2), s3 = max(klo, B3);
    const unsigned k0 = (unsigned)__builtin_amdgcn_ballot_w64(klo < min(khi, B1));
    const unsigned k1 = (unsigned)(__builtin_amdgcn_ballot_w64(klo < min(khi, l10)) | __builtin_amdgcn_ballot_w64(s1 < min(khi, B2)));
    const unsigned k2 = (unsigned)(__builtin_amdgcn_ballot_w64(klo < min(khi, l20)) | __builtin_amdgcn_ballot_w64(s1 + P1 < min(khi + P1, B1 + l21)) |
                                   __builtin_amdgcn_ballot_w64(s2 < min(khi, B3)));
    const unsigned k3 = (unsigned)(__builtin_amdgcn_ballot_w64(klo < min(khi, l30)) | __builtin_amdgcn_ballot_w64(s1 + P1 < min(khi + P1, B1 + l31)) |
                                   __builtin_amdgcn_ballot_w64(s2 + P2 < min(khi + P2, B2 + l32)) | __builtin_amdgcn_ballot_w64(s3 < khi));
    const unsigned cb = crow >> 2;
    cmask = cb == 0 ? k0 : cb == 1 ? k1 : cb == 2 ? k2 : k3;
    publish = (((wave == 0 ? k0 : wave == 1 ? k1 : wave == 2 ? k2 : k3) >> split) & 1u) != 0;
  }
  const unsigned tag_pub = ep_pub + 1u, tag_own = ep_own + 1u;
  unsigned want[8], doff[8];
  const unsigned cmg = ~(cmask >> g);
#pragma unroll
  for (int s = 0; s < 8; ++s) {
    const unsigned off = (cmg << (31 - 4 * s)) & OOB;
    want[s] = off ? 0u : tag_own;
    doff[s] = off | (rb_own + RB_PO + (4 * s + g) * 512 + wave * 128 + c * 8);
    asm volatile("" : "+v"(want[s]), "+v"(doff[s]));
  }
  const unsigned lso = (cmg << (31 - 4 * (c & 7))) & OOB;
  unsigned lwant = lso ? 0u : tag_own, loff = lso | (rb_own + RB_LSE + (4 * (c & 7) + g) * 8);
  float lbias = lso ? NEG_BIG : 0.f;
  asm volatile("" : "+v"(lwant), "+v"(loff), "+v"(lbias));
  if ((int)ctx4[0] <= 0 || (int)ctx4[1] <= 0 || (int)ctx4[2] <= 0 || (int)ctx4[3] <= 0) {
    const int l = myb == 0 ? ctx4[0] : myb == 1 ? ctx4[1] : myb == 2 ? ctx4[2] : ctx4[3];
#pragma unroll
    for (int s = 0; s < 4; ++s) qf[s] = l <= 0 ? u32x4{0, 0, 0, 0} : qf[s];
  }

  float m = NEG_BIG, l = 0.f;
  f32x4 acc[8];
#pragma unroll
  for (int t = 0; t < 8; ++t) acc[t] = f32x4{0.f, 0.f, 0.f, 0.f};
  const unsigned csh = 8 * myb;
  u32x4* kst = reinterpret_cast<u32x4*>(ldsu + LDS_K + wave * 1024);
  STAMP(7);

  for (int i = 0; i < n; ++i) {
    const __amdgpu_buffer_rsrc_t vrs0 = rsrc(vc, i), krs1 = rsrc(kc, i + 1), vrs1 = rsrc(vc, i + 1);
    const unsigned so0 = __builtin_amdgcn_readlane(item0, i) * BLK_BYTES, so1 = __builtin_amdgcn_readlane(item0, i + 1) * BLK_BYTES;
    auto next = [&](int x) {
      const int y = x + WIN;
      __builtin_amdgcn_sched_barrier(0);
      if (y < 8) vr[y - 4] = __builtin_amdgcn_raw_buffer_load_b128(vrs0, GOFF(y - 4), so0, KV_AUX);
      else if (y < 12) kr[y - 8] = __builtin_amdgcn_raw_buffer_load_b128(krs1, GOFF(y - 8), so1, KV_AUX);
      else vr[y - 12] = __builtin_amdgcn_raw_buffer_load_b128(vrs1, GOFF(y - 12), so1, KV_AUX);
      __builtin_amdgcn_sched_barrier(0);
    };
#pragma unroll
    for (int j = 0; j < 4; ++j) { kst[(4 * g + j) * 16 + (c ^ (4 * g + j))] = kr[j]; next(j); }
    const unsigned cn = __builtin_amdgcn_readlane(item1, i);
    f32x4 sc = f32x4{0.f, 0.f, 0.f, 0.f};
#pragma unroll
    for (int s = 0; s < 4; ++s) {
      const u32x4 kf = kst[c * 16 + ((4 * s + g) ^ c)];
      sc = __builtin_amdgcn_mfma_f32_16x16x32_bf16(__builtin_bit_cast(bf16x8, kf), __builtin_bit_cast(bf16x8, qf[s]), sc, 0, 0, 0);
    }
    const int nv = (int)((cn >> csh) & 0xffu) - 4 * (int)g;
    float x[4];
#pragma unroll
    for (int r = 0; r < 4; ++r) x[r] = r < nv ? sc[r] * C2 : -INFINITY;
    const float mn = fmaxf(m, xg_max(fmaxf(fmaxf(x[0], x[1]), fmaxf(x[2], x[3]))));
    const float alpha = __builtin_amdgcn_exp2f(m - mn);
    m = mn;
    float p[4];
#pragma unroll
    for (int r = 0; r < 4; ++r) p[r] = __builtin_amdgcn_exp2f(x[r] - mn);
    l = l * alpha + ((p[0] + p[1]) + (p[2] + p[3]));
    u32x2 pk = u32x2{cvtpk(p[0], p[1]), cvtpk(p[2], p[3])};
#pragma unroll
    for (int t = 0; t < 8; ++t) { acc[t] *= alpha; asm volatile("" : "+v"(acc[t])); }
    asm volatile("" : "+v"(pk));
    __builtin_amdgcn_sched_barrier(0);
    u32x2 vt[8];
#pragma unroll
    for (int pr = 0; pr < 2; ++pr) {
      if (WIN < 8) { asm volatile("" :: "v"(vr[2 * pr])); next(4 + 2 * pr); }
#pragma unroll
      for (int t = 0; t < 8; ++t) {
        vt[t][pr] = __builtin_amdgcn_perm(vr[2 * pr + 1][t >> 1], vr[2 * pr][t >> 1], (t & 1) ? 0x07060302u : 0x05040100u);
        if (pr == 1) asm volatile("" : "+v"(vt[t])); else { unsigned z = vt[t][0]; asm volatile("" : "+v"(z)); vt[t][0] = z; }
      }
      if (WIN == 8) next(4 + 2 * pr);
      next(5 + 2 * pr);
    }
#pragma unroll
    for (int t = 0; t < 8; ++t)
      acc[t] = __builtin_amdgcn_mfma_f32_16x16x16bf16_1k(__builtin_bit_cast(s16x4, vt[t]), __builtin_bit_cast(s16x4, pk), acc[t], 0, 0, 0);
  }
  STAMP(2);

  l = xg_sum(l);
  {
    float* row = lds + LDS_RED + (wave * 16 + c) * RED_ROW + 32 * g;
    if (g == 0) *reinterpret_cast<f32x2*>(lds + LDS_ML + (c * NWAVE + wave) * 2) = f32x2{m, l};
#pragma unroll
    for (int r = 0; r < 4; ++r) {
      *reinterpret_cast<f32x4*>(row + 8 * r) = f32x4{acc[0][r], acc[1][r], acc[2][r], acc[3][r]};
      *reinterpret_cast<f32x4*>(row + 8 * r + 4) = f32x4{acc[4][r], acc[5][r], acc[6][r], acc[7][r]};
    }
  }
  __syncthreads();
  STAMP(3);
  if (publish) {
    const unsigned r = 4 * wave + g;
    const f32x4 ml0 = *reinterpret_cast<const f32x4*>(lds + LDS_ML + r * NWAVE * 2);
    const f32x4 ml1 = *reinterpret_cast<const f32x4*>(lds + LDS_ML + r * NWAVE * 2 + 4);
    f32x4 a[NWAVE][2];
#pragma unroll
    for (int w = 0; w < NWAVE; ++w) {
      a[w][0] = *reinterpret_cast<const f32x4*>(lds + LDS_RED + (w * 16 + r) * RED_ROW + 8 * c);
      a[w][1] = *reinterpret_cast<const f32x4*>(lds + LDS_RED + (w * 16 + r) * RED_ROW + 8 * c + 4);
    }
    __builtin_amdgcn_sched_barrier(0);
    const float M = fmaxf(fmaxf(ml0[0], ml0[2]), fmaxf(ml1[0], ml1[2]));
    float f0 = __builtin_amdgcn_exp2f(ml0[0] - M), f1 = __builtin_amdgcn_exp2f(ml0[2] - M);
    float f2 = __builtin_amdgcn_exp2f(ml1[0] - M), f3 = __builtin_amdgcn_exp2f(ml1[2] - M);
    const float L = f0 * ml0[1] + f1 * ml0[3] + f2 * ml1[1] + f3 * ml1[3];
    const float inv = __builtin_amdgcn_rcpf(L);
    f0 *= inv; f1 *= inv; f2 *= inv; f3 *= inv;
    const unsigned po = rb_pub + RB_PO + c * 16;
#pragma unroll
    for (int i = 0; i < 2; ++i) {
      const f32x4 o = f0 * a[0][i] + f1 * a[1][i] + f2 * a[2][i] + f3 * a[3][i];
      __builtin_amdgcn_raw_buffer_store_b128(u32x4{cvtpk(o[0], o[1]), tag_pub, cvtpk(o[2], o[3]), tag_pub}, wsr, po + i * 256, split * 512, AUX_SC1);
    }
    const float lse = M + __builtin_amdgcn_logf(L);
    __builtin_amdgcn_raw_buffer_store_b64(u32x2{__float_as_uint(lse), tag_pub}, wsr, c == 0 ? rb_pub + RB_LSE : OOB, split * 8, AUX_SC1);
  }
  STAMP(4);
  if (!consumer) { STAMP_FLUSH(); return; }

  {
    u32x2 lg, x[8];
    unsigned spins = 0;
    if (POLL_MODE == 2) {
      if (wave == 0) {
#pragma unroll 1
        for (;;) {
          asm volatile("buffer_load_dwordx2 %0, %1, %2, 0 offen sc1\n\ts_waitcnt vmcnt(0)" : "=&v"(lg) : "v"(loff), "s"(wsr) : "memory");
          if (__builtin_amdgcn_ballot_w64(lg[1] != lwant) == 0 || ++spins > SPIN_CAP) break;
          if (POLL_SLEEP) __builtin_amdgcn_s_sleep(POLL_SLEEP);
        }
      }
      __builtin_amdgcn_s_barrier();
    }
    STAMP(8);
#pragma unroll 1
    for (;;) {
      asm volatile("buffer_load_dwordx2 %0, %9, %18, 0 offen sc1\n\t"
                   "buffer_load_dwordx2 %1, %10, %18, 0 offen sc1\n\tbuffer_load_dwordx2 %2, %11, %18, 0 offen sc1\n\t"
                   "buffer_load_dwordx2 %3, %12, %18, 0 offen sc1\n\tbuffer_load_dwordx2 %4, %13, %18, 0 offen sc1\n\t"
                   "buffer_load_dwordx2 %5, %14, %18, 0 offen sc1\n\tbuffer_load_dwordx2 %6, %15, %18, 0 offen sc1\n\t"
                   "buffer_load_dwordx2 %7, %16, %18, 0 offen sc1\n\tbuffer_load_dwordx2 %8, %17, %18, 0 offen sc1\n\t"
                   "s_waitcnt vmcnt(0)"
                   : "=&v"(lg), "=&v"(x[0]), "=&v"(x[1]), "=&v"(x[2]), "=&v"(x[3]), "=&v"(x[4]), "=&v"(x[5]), "=&v"(x[6]), "=&v"(x[7])
                   : "v"(loff), "v"(doff[0]), "v"(doff[1]), "v"(doff[2]), "v"(doff[3]), "v"(doff[4]), "v"(doff[5]), "v"(doff[6]), "v"(doff[7]), "s"(wsr)
                   : "memory");
      unsigned bad = lg[1] ^ lwant;
#pragma unroll
      for (int s = 0; s < 8; ++s) { bad |= x[s][1] ^ want[s]; asm volatile("" : "+v"(bad)); }
      if (__builtin_amdgcn_ballot_w64(bad != 0) == 0 || ++spins > SPIN_CAP) break;
      if (POLL_SLEEP) __builtin_amdgcn_s_sleep(POLL_SLEEP);
    }
    STAMP(5);
    const float lse = __uint_as_float(lg[0]) + lbias;
    float M = fmaxf(lse, dppf<XOR1>(lse)); M = fmaxf(M, dppf<XOR2>(M)); M = fmaxf(M, dppf<ROR4>(M)); M = xg_max(M);
    const float wk = __builtin_amdgcn_exp2f(lse - M);
    float L = wk + dppf<XOR1>(wk); L += dppf<XOR2>(L); L += dppf<ROR4>(L);
    float o0 = 0.f, o1 = 0.f;
#define ACC_SLOT(S) { const float w_ = dppf<BCAST + S>(wk); o0 = __builtin_fmaf(w_, __uint_as_float(x[S][0] << 16), o0); o1 = __builtin_fmaf(w_, __uint_as_float(x[S][0] & 0xffff0000u), o1); }
    ACC_SLOT(0) ACC_SLOT(1) ACC_SLOT(2) ACC_SLOT(3) ACC_SLOT(4) ACC_SLOT(5) ACC_SLOT(6) ACC_SLOT(7)
#undef ACC_SLOT
    const float inv = __builtin_amdgcn_rcpf(xg_sum(L));
    o0 = xg_sum(o0) * inv; o1 = xg_sum(o1) * inv;
    const unsigned orow = (crow >> 2) * H + 4 * h + (crow & 3);
    if (g == 0) *reinterpret_cast<unsigned*>(out + orow * D + 64 * (wave & 1) + 8 * (c >> 1) + 4 * (wave >> 1) + 2 * (c & 1)) = cvtpk(o0, o1);
    if (tid == 0) __builtin_amdgcn_raw_buffer_store_b32(tag_own, wsr, 0, rb_own, AUX_SC1);
    STAMP(6);
    STAMP_FLUSH();
  }
}

}

extern "C" void kernel_launch(void* const* d_in, const int* in_sizes, int n_in, void* d_out, int out_size, void* d_ws, size_t ws_size,
                              hipStream_t stream) {
  attn_decode<<<HKV * NSPLIT, NTHR, 0, stream>>>((const int*)d_in[3], (const int*)d_in[4], (const unsigned short*)d_in[0], (unsigned char*)d_ws,
                                                 (const unsigned short*)d_in[1], (const unsigned short*)d_in[2], (unsigned short*)d_out,
                                                 (unsigned long long)in_sizes[1] * 2ull);
}
```

```cpp
#include <hip/hip_runtime.h>
#include <hip/hip_bf16.h>
#include <cstdint>
#include <type_traits>

namespace {

constexpr int B = 4, H = 32, HKV = 8, D = 128, BS = 16, NB = 1024;
constexpr unsigned LK = NB * BS;
constexpr int NSPLIT = 32;
constexpr int NWAVE = 4, NTHR = NWAVE * 64;
constexpr unsigned NEARLY = NSPLIT / B;
#define KV_AUX 2
#define ROT 0
#define POLL_MODE 2
#define POLL_SLEEP 1
#define POLL_SLEEP1 1
#define SLK 3
#define SLV 3
#define PACE 1075
#define PLAG 450
#define STAG 2
#define HDELAY 2
#define CWAKE 2000
#define HINT_AT 0
#define CSHIFT 1
#define CODE_PF 1
#define LATE 1
#define TB_AUX 0
#define MAPMODE 0
#define POLL_IDLE 12
#define LSTR 8
#define ONEPOLL 62
#define SKEW 0
#define GAIN_IOD 25
#define GAIN_XCD 80
#define SPIN_CAP (1u << 18)
constexpr float C2 = 0.088388347648318440f * 1.4426950408889634f;
constexpr int BLK_BYTES = BS * HKV * D * 2;
constexpr float NEG_BIG = -3.0e38f;
constexpr int AUX_SC1 = 16;
constexpr unsigned OOB = 0x80000000u;

using u32x4 = __attribute__((ext_vector_type(4))) unsigned;
using u32x2 = __attribute__((ext_vector_type(2))) unsigned;
using u64x2 = __attribute__((ext_vector_type(2))) unsigned long long;
using f32x4 = __attribute__((ext_vector_type(4))) float;
using f32x2 = __attribute__((ext_vector_type(2))) float;
using bf16x8 = __attribute__((ext_vector_type(8))) __bf16;
using bf16x2 = __attribute__((ext_vector_type(2))) __bf16;
using s16x4 = __attribute__((ext_vector_type(4))) short;
typedef __attribute__((address_space(4))) const unsigned cu32;
typedef __attribute__((address_space(4))) const u32x4 cu32x4;
typedef __attribute__((address_space(1))) const unsigned gu32;

constexpr unsigned RB_LSE = 256, RB_PO = RB_LSE + NSPLIT * LSTR, RB_BYTES = RB_PO + NSPLIT * 512;
constexpr unsigned WS_ST = B * H * RB_BYTES;
constexpr unsigned WS_USED = WS_ST + 256;
constexpr size_t WS_DBG = WS_USED;

constexpr int RED_ROW = D + 4;
constexpr int LDS_LCP = 0;
constexpr int LDS_TK = 64;
constexpr int LDS_ML = 128;
constexpr int LDS_F = LDS_ML + NWAVE * 16 * 2;
constexpr int LDS_K = LDS_F + 2 * 16 * NWAVE;
constexpr int LDS_TAB = LDS_K + NWAVE * 1024;
constexpr int LDS_RED = LDS_TAB + B * NB;
constexpr int LDS_WORDS = LDS_RED + NWAVE * 16 * RED_ROW;

__device__ __forceinline__ unsigned cvtpk(float lo, float hi) {
  return __builtin_bit_cast(unsigned, __builtin_convertvector(f32x2{lo, hi}, bf16x2));
}
__device__ __forceinline__ float xg_max(float x) {
  auto s = __builtin_amdgcn_permlane16_swap(__float_as_uint(x), __float_as_uint(x), false, false);
  x = fmaxf(__uint_as_float(s[0]), __uint_as_float(s[1]));
  auto t = __builtin_amdgcn_permlane32_swap(__float_as_uint(x), __float_as_uint(x), false, false);
  return fmaxf(__uint_as_float(t[0]), __uint_as_float(t[1]));
}
__device__ __forceinline__ float xg_sum(float x) {
  auto s = __builtin_amdgcn_permlane16_swap(__float_as_uint(x), __float_as_uint(x), false, false);
  x = __uint_as_float(s[0]) + __uint_as_float(s[1]);
  auto t = __builtin_amdgcn_permlane32_swap(__float_as_uint(x), __float_as_uint(x), false, false);
  return __uint_as_float(t[0]) + __uint_as_float(t[1]);
}
template <int CTRL> __device__ __forceinline__ float dppf(float x) {
  return __builtin_bit_cast(float, __builtin_amdgcn_mov_dpp(__builtin_bit_cast(int, x), CTRL, 0xf, 0xf, true));
}
constexpr int XOR1 = 0xB1, XOR2 = 0x4E, ROR4 = 0x124, BCAST = 0x150;
template <int LANE>
__device__ __forceinline__ void first_diff(unsigned& cand, u32x4 a, u32x4 b) {
  const u64x2 x = __builtin_bit_cast(u64x2, a), y = __builtin_bit_cast(u64x2, b);
  const unsigned long long mk = __builtin_amdgcn_ballot_w64(x[0] != y[0]) | __builtin_amdgcn_ballot_w64(x[1] != y[1]);
  unsigned r; asm("s_ff1_i32_b64 %1, %2\n\tv_writelane_b32 %0, %1, %3" : "+v"(cand), "=&s"(r) : "s"(mk), "i"(LANE));
}
__device__ __forceinline__ unsigned seq_len(unsigned c) { return min(c - 1u, LK - 1u) + 1u; }
__device__ __forceinline__ unsigned now() { return (unsigned)__builtin_amdgcn_s_memrealtime(); }
#define STAMP(i) do {} while (0)
#define STAMP_FLUSH() do {} while (0)

__global__ void __attribute__((aligned(4096))) __launch_bounds__(NTHR, 2)
attn_decode(const int* __restrict__ bt, const int* __restrict__ ctx, const unsigned short* __restrict__ q, unsigned char* ws,
            const unsigned short* __restrict__ kc, const unsigned short* __restrict__ vc, unsigned short* __restrict__ out, unsigned long long kv_bytes64) {
  __shared__ __attribute__((aligned(16))) float lds[LDS_WORDS];
  unsigned* ldsu = reinterpret_cast<unsigned*>(lds);
  char* ldsb = reinterpret_cast<char*>(lds);
  STAMP(0);
  const unsigned tid = threadIdx.x, lane = tid & 63, wave = __builtin_amdgcn_readfirstlane(tid >> 6);
  const unsigned g = lane >> 4, c = lane & 15;
  const bool combiner = blockIdx.x >= HKV * NSPLIT;
  const unsigned split = (MAPMODE == 0 || combiner) ? blockIdx.x >> 3 : MAPMODE == 1 ? (blockIdx.x & 7) * 4 + (blockIdx.x >> 6) : (blockIdx.x & 7) + 8 * (blockIdx.x >> 6);
  const unsigned h = (MAPMODE == 0 || combiner) ? (blockIdx.x + (blockIdx.x >> 8) * CSHIFT) & 7 : (blockIdx.x >> 3) & 7;
  const unsigned kv_bytes = (unsigned)kv_bytes64;
  const unsigned t_in = now();
  unsigned goff = g * 8192 + h * 256 + 16 * c;
  unsigned goff2 = goff + 4096;
  ldsu[LDS_LCP + lane] = 0xffffffffu; ldsu[LDS_TK + lane] = 0xffffffffu;
  asm volatile("s_waitcnt lgkmcnt(0)" : "+v"(goff), "+v"(goff2) : "s"(bt), "s"(ctx), "s"(q), "s"(ws), "s"(kc), "s"(vc), "s"(out), "s"(kv_bytes64) : "memory");
  if (__builtin_expect(combiner, 0)) {
    const __amdgpu_buffer_rsrc_t wsr = __builtin_amdgcn_make_buffer_rsrc((void*)ws, 0, WS_USED, 0x00020000);
    const __amdgpu_buffer_rsrc_t outr = __builtin_amdgcn_make_buffer_rsrc((void*)out, 0, B * H * D * 2, 0x00020000);
    const unsigned crow = split - NSPLIT, rb_own = (h * 16 + crow) * RB_BYTES;
    if (SKEW && crow == 0) __builtin_amdgcn_raw_buffer_store_b32(t_in, wsr, tid == 0 ? WS_ST : OOB, (__builtin_amdgcn_s_getreg(20 | (2 << 11)) & 7) * 4, AUX_SC1);
    const unsigned tag_own = __builtin_amdgcn_raw_buffer_load_b32(wsr, 0, rb_own, AUX_SC1) + 1u;
    const unsigned loff = rb_own + RB_LSE + (4 * (c & 7) + g) * LSTR;
    const unsigned cp = 8 * (wave & 1) + (c >> 1);
    unsigned ooff = g == 0 ? 2 * (32 * (cp & 3) + 8 * (2 * (wave >> 1) + (c & 1)) + 2 * (cp >> 2)) : OOB;
    unsigned dbase = rb_own + RB_PO + g * 512 + wave * 128 + c * 8;
    asm volatile("s_barrier" : "+v"(ooff), "+v"(dbase) : "v"(tag_own) : "memory");
    unsigned db[4];
#pragma unroll
    for (int s = 0; s < 4; ++s) { db[s] = dbase + s * 4096; asm volatile("" : "+v"(db[s])); }
    u32x2 lg, x[8];
    unsigned spins = 0;
    if (ONEPOLL && wave != 0) __builtin_amdgcn_s_barrier();
    bool seen = !ONEPOLL || wave != 0;
#pragma unroll 1
    for (;;) {
      asm volatile("buffer_load_dwordx2 %0, %1, %2, 0 offen sc1\n\ts_waitcnt vmcnt(0)" : "=&v"(lg) : "v"(loff), "s"(wsr) : "memory");
      const unsigned long long miss = __builtin_amdgcn_ballot_w64(lg[1] != tag_own);
      ++spins;
      if (!seen && (__builtin_popcountll(miss) <= ONEPOLL || spins > SPIN_CAP)) { seen = true; __builtin_amdgcn_s_barrier(); }
      if (miss == 0 || spins > SPIN_CAP) break;
      if (miss == ~0ull) __builtin_amdgcn_s_sleep(POLL_IDLE);
      else if (POLL_SLEEP1) __builtin_amdgcn_s_sleep(POLL_SLEEP1);
    }
    if (HDELAY) __builtin_amdgcn_s_sleep(HDELAY);
    STAMP(8);
    const float lse = __uint_as_float(lg[0]);
    const int absent = lse == NEG_BIG ? (int)OOB : 0;
    unsigned doff[8];
#define DOFF(S) doff[S] = (unsigned)__builtin_amdgcn_mov_dpp(absent, BCAST + S, 0xf, 0xf, true) | db[S >> 1];
    DOFF(0) DOFF(1) DOFF(2) DOFF(3) DOFF(4) DOFF(5) DOFF(6) DOFF(7)
#undef DOFF
#define LOAD_ROWS() asm volatile("buffer_load_dwordx2 %0, %8, %16, 0 offen sc1\n\tbuffer_load_dwordx2 %1, %9, %16, 0 offen offset:2048 sc1\n\t" \
                   "buffer_load_dwordx2 %2, %10, %16, 0 offen sc1\n\tbuffer_load_dwordx2 %3, %11, %16, 0 offen offset:2048 sc1\n\t" \
                   "buffer_load_dwordx2 %4, %12, %16, 0 offen sc1\n\tbuffer_load_dwordx2 %5, %13, %16, 0 offen offset:2048 sc1\n\t" \
                   "buffer_load_dwordx2 %6, %14, %16, 0 offen sc1\n\tbuffer_load_dwordx2 %7, %15, %16, 0 offen offset:2048 sc1" \
                   : "=&v"(x[0]), "=&v"(x[1]), "=&v"(x[2]), "=&v"(x[3]), "=&v"(x[4]), "=&v"(x[5]), "=&v"(x[6]), "=&v"(x[7]) \
                   : "v"(doff[0]), "v"(doff[1]), "v"(doff[2]), "v"(doff[3]), "v"(doff[4]), "v"(doff[5]), "v"(doff[6]), "v"(doff[7]), "s"(wsr) \
                   : "memory")
    LOAD_ROWS();
    __builtin_amdgcn_sched_barrier(0);
    float w[8]; unsigned want[8];
    {
      float M = fmaxf(lse, dppf<XOR1>(lse)); M = fmaxf(M, dppf<XOR2>(M)); M = fmaxf(M, dppf<ROR4>(M)); M = xg_max(M);
      const float wk = __builtin_amdgcn_exp2f(lse - M);
      float L = wk + dppf<XOR1>(wk); L += dppf<XOR2>(L); L += dppf<ROR4>(L);
      const float wn = wk * __builtin_amdgcn_rcpf(xg_sum(L));
      w[0] = dppf<BCAST + 0>(wn); w[1] = dppf<BCAST + 1>(wn); w[2] = dppf<BCAST + 2>(wn); w[3] = dppf<BCAST + 3>(wn);
      w[4] = dppf<BCAST + 4>(wn); w[5] = dppf<BCAST + 5>(wn); w[6] = dppf<BCAST + 6>(wn); w[7] = dppf<BCAST + 7>(wn);
#pragma unroll
      for (int s = 0; s < 8; ++s) { want[s] = (int)doff[s] < 0 ? 0u : tag_own; asm volatile("" : "+v"(w[s]), "+v"(want[s])); }
    }
#pragma unroll 1
    for (;;) {
      asm volatile("s_waitcnt vmcnt(0)" : "+v"(x[0]), "+v"(x[1]), "+v"(x[2]), "+v"(x[3]), "+v"(x[4]), "+v"(x[5]), "+v"(x[6]), "+v"(x[7]) :: "memory");
      unsigned bad = x[0][1] ^ want[0];
#pragma unroll
      for (int s = 1; s < 8; ++s) bad = __builtin_amdgcn_bitop3_b32(x[s][1], want[s], bad, 0xBE);
      if (__builtin_expect(__builtin_amdgcn_ballot_w64(bad != 0) == 0, 1) || ++spins > SPIN_CAP) break;
      LOAD_ROWS();
    }
#undef LOAD_ROWS
    STAMP(5);
    float o0 = 0.f, o1 = 0.f;
#pragma unroll
    for (int s = 0; s < 8; ++s) {
      o0 = __builtin_fmaf(w[s], __uint_as_float(x[s][0] << 16), o0); o1 = __builtin_fmaf(w[s], __uint_as_float(x[s][0] & 0xffff0000u), o1);
    }
    o0 = xg_sum(o0); o1 = xg_sum(o1);
    __builtin_amdgcn_raw_buffer_store_b32(cvtpk(o0, o1), outr, ooff, ((crow >> 2) * H + 4 * h + (crow & 3)) * D * 2, 0);
    if (tid == 0) __builtin_amdgcn_raw_buffer_store_b32(tag_own, wsr, 0, rb_own, AUX_SC1);
    if (blockIdx.x == 256) { while ((int)(now() - t_in) < 6000 + (int)((__builtin_amdgcn_s_getreg(20 | (2 << 11)) & 7) * 400)) __builtin_amdgcn_s_sleep(1); }
    return;
  }

  const unsigned eb = (split >> 3) & 3;
  unsigned ek = (split & 7) * NWAVE + 3 - wave, erow = eb * NB * 4, kvb = kv_bytes;
  unsigned ectx = ((cu32*)ctx)[eb];
  u32x4 ctx4 = *(cu32x4*)ctx;
  const __amdgpu_buffer_rsrc_t btr = __builtin_amdgcn_make_buffer_rsrc((void*)bt, 0, B * NB * 4, 0x00020000);
  u32x4 tb[B];
#pragma unroll
  for (int b = 0; b < B; ++b) tb[b] = __builtin_amdgcn_raw_buffer_load_b128(btr, 16 * tid, b * NB * 4, TB_AUX);
  unsigned pf = 0;
  if (CODE_PF) {
    unsigned long long pc; asm volatile("s_getpc_b64 %0" : "=s"(pc));
    pf = ((gu32*)(pc & ~4095ull))[32 * lane];
  }
  __builtin_amdgcn_sched_barrier(0);
  u32x4 qf[4];
  const unsigned myb = c >> 2;
  const __amdgpu_buffer_rsrc_t qr = __builtin_amdgcn_make_buffer_rsrc((void*)q, 0, B * H * D * 2, 0x00020000);
  const unsigned qoff = ((myb * H + (c & 3)) * D + 8 * g) * 2;
  if (!(LATE & 2)) {
#pragma unroll
    for (int s = 0; s < 4; ++s) qf[s] = __builtin_amdgcn_raw_buffer_load_b128(qr, qoff + 64 * s, 4 * h * D * 2, TB_AUX);
  }
  const __amdgpu_buffer_rsrc_t wsr = __builtin_amdgcn_make_buffer_rsrc((void*)ws, 0, WS_USED, 0x00020000);
  const unsigned rb_pub = (h * 16 + 4 * wave + g) * RB_BYTES, rb_row = (h * 16 + c) * RB_BYTES;
  unsigned ep_pub, ep_row;
  if (!(LATE & 1)) { ep_pub = __builtin_amdgcn_raw_buffer_load_b32(wsr, rb_pub, 0, AUX_SC1); ep_row = __builtin_amdgcn_raw_buffer_load_b32(wsr, rb_row, 0, AUX_SC1); }
  float m = NEG_BIG, l = 0.f;
  f32x4 acc[8];
#pragma unroll
  for (int t = 0; t < 8; ++t) { acc[t] = f32x4{0.f, 0.f, 0.f, 0.f}; asm volatile("" : "+v"(acc[t])); }
  unsigned csh = 8 * myb, g4 = 4 * g;
  unsigned kw[4], kd[4];
#pragma unroll
  for (int j = 0; j < 4; ++j) {
    kw[j] = (LDS_K + wave * 1024) * 4 + ((4 * g + j) * 16 + (c ^ (4 * g + j))) * 16;
    kd[j] = (LDS_K + wave * 1024) * 4 + (c * 16 + ((4 * j + g) ^ c)) * 16;
    asm volatile("" : "+v"(kw[j]), "+v"(kd[j]));
  }
  unsigned red = (LDS_RED + (wave * 16 + c) * RED_ROW + 4 * g) * 4;
  asm volatile("" : "+v"(csh), "+v"(g4), "+v"(red), "+s"(ek), "+s"(erow), "+s"(kvb));
  __builtin_amdgcn_sched_barrier(0);
  asm volatile("" : "+s"(ectx));
  const unsigned elen = seq_len(ectx);
  const int elast = (int)((elen - 1u) / BS), ecol = (elast | 3) - (int)ek;
  unsigned ephys;
  asm volatile("s_load_dword %0, %1, %2 offset:0" : "=s"(ephys) : "s"(bt), "s"(erow + 4 * min(max(ecol, 0), elast)));
  __builtin_amdgcn_sched_barrier(0);
  asm volatile("" : "+s"(ctx4));
  unsigned na0 = (seq_len(ctx4[0]) + 4 * BS - 1) / (4 * BS), na1 = (seq_len(ctx4[1]) + 4 * BS - 1) / (4 * BS);
  unsigned na2 = (seq_len(ctx4[2]) + 4 * BS - 1) / (4 * BS), na3 = (seq_len(ctx4[3]) + 4 * BS - 1) / (4 * BS);
  unsigned nc0 = max(na0, NEARLY) - NEARLY, nc1 = max(na1, NEARLY) - NEARLY;
  unsigned nc2 = max(na2, NEARLY) - NEARLY, nc3 = max(na3, NEARLY) - NEARLY;
  unsigned e1 = (ecol >= 0 && ecol <= elast) ? min(elen - BS * ecol, (unsigned)BS) << (8 * eb) : 0u;
  asm volatile("" : "+v"(nc0), "+v"(nc1), "+v"(nc2), "+v"(nc3), "+s"(e1));
  __builtin_amdgcn_sched_barrier(0);
  asm volatile("s_waitcnt lgkmcnt(0)" : "+s"(ephys));
  const unsigned tb0 = now();
  __builtin_amdgcn_sched_barrier(0);
  STAMP(10);
#define GOFF(j) (((j) & 2 ? goff2 : goff) + 2048 * ((j) & 1))
  u32x4 kr[4], vr[4];
  const __amdgpu_buffer_rsrc_t krs = __builtin_amdgcn_make_buffer_rsrc((void*)kc, 0, kvb, 0x00020000);
  const __amdgpu_buffer_rsrc_t vrs = __builtin_amdgcn_make_buffer_rsrc((void*)vc, 0, kvb, 0x00020000);
#pragma unroll
  for (int j = 0; j < 4; ++j) kr[j] = __builtin_amdgcn_raw_buffer_load_b128(krs, GOFF(j), ephys * BLK_BYTES, KV_AUX);
#pragma unroll
  for (int j = 0; j < 4; ++j) vr[j] = __builtin_amdgcn_raw_buffer_load_b128(vrs, GOFF(j), ephys * BLK_BYTES, KV_AUX);
  __builtin_amdgcn_sched_barrier(0);
  if (LATE & 2) {
#pragma unroll
    for (int s = 0; s < 4; ++s) qf[s] = __builtin_amdgcn_raw_buffer_load_b128(qr, qoff + 64 * s, 4 * h * D * 2, TB_AUX);
  }
  if (LATE & 1) { ep_pub = __builtin_amdgcn_raw_buffer_load_b32(wsr, rb_pub, 0, AUX_SC1); ep_row = __builtin_amdgcn_raw_buffer_load_b32(wsr, rb_row, 0, AUX_SC1); }
  __builtin_amdgcn_sched_barrier(0);
  STAMP(1);

  asm volatile("s_barrier" ::: "memory");
#pragma unroll
  for (int b = 0; b < B; ++b) *reinterpret_cast<u32x4*>(ldsu + LDS_TAB + b * NB + 4 * tid) = tb[b];
  {
    unsigned cand = 0xffffffffu;
    first_diff<0>(cand, tb[1], tb[0]);
    first_diff<1>(cand, tb[2], tb[0]);
    first_diff<2>(cand, tb[3], tb[0]);
    first_diff<3>(cand, tb[2], tb[1]);
    first_diff<4>(cand, tb[3], tb[1]);
    first_diff<5>(cand, tb[3], tb[2]);
    __hip_atomic_fetch_min(ldsu + LDS_LCP + lane, cand | (wave << 6), __ATOMIC_RELAXED, __HIP_MEMORY_SCOPE_WORKGROUP);
  }
  asm volatile("s_waitcnt lgkmcnt(0)\n\ts_barrier" ::: "memory");
  STAMP(9);
  const u32x4 la = *reinterpret_cast<const u32x4*>(ldsu + LDS_LCP);
  const u32x2 lb = *reinterpret_cast<const u32x2*>(ldsu + LDS_LCP + 4);
  const unsigned l10 = min(la[0], min(nc0, nc1)), l20 = min(la[1], min(nc0, nc2)), l30 = min(la[2], min(nc0, nc3));
  const unsigned l21 = min(la[3], min(nc1, nc2)), l31 = min(lb[0], min(nc1, nc3)), l32 = min(lb[1], min(nc2, nc3));
  const unsigned P1 = l10, P2 = max(l20, l21), P3 = max(l30, max(l31, l32));
  const unsigned B1 = nc0, B2 = B1 + nc1 - P1, B3 = B2 + nc2 - P2, total = B3 + nc3 - P3;
  const unsigned lo = (total * split) / NSPLIT, nm = (total * split + total) / NSPLIT - lo;
  unsigned li = lane - 1u;
  if (ROT) { li += min(ROT == 1 ? split >> 1 : ROT == 2 ? (3 * split >> 1) % 15 : 0u, nm); li = min(li, li - nm); }
  const unsigned pos = lo + li;
  unsigned idx = pos;
  idx = pos >= B1 ? pos + (NB / 4 + P1 - B1) : idx;
  idx = pos >= B2 ? pos + (2 * NB / 4 + P2 - B2) : idx;
  idx = pos >= B3 ? pos + (3 * NB / 4 + P3 - B3) : idx;
  idx &= B * NB / 4 - 1;
  const bool live = lane - 1u < nm;
  unsigned item0 = ldsu[LDS_TAB + 4 * idx + wave];
  item0 = live ? item0 : 0u;
  const int n = max(__builtin_amdgcn_readfirstlane(nm), 1) + 1;
  unsigned item1;
  {
    const unsigned ib = idx >> 8, j = idx & (NB / 4 - 1);
    const unsigned x1 = ib == 0 ? l10 : 0u, x2 = ib == 0 ? l20 : ib == 1 ? l21 : 0u, x3 = ib == 0 ? l30 : ib == 1 ? l31 : l32;
    item1 = (16u << (8 * ib)) | (j < x1 ? 16u << 8 : 0u) | (j < x2 ? 16u << 16 : 0u) | (j < x3 ? 16u << 24 : 0u);
    item1 = live ? item1 : 0u;
    item1 = lane == 0 ? e1 : item1;
  }
  const unsigned pm = (__builtin_amdgcn_ballot_w64((item1 & 0xffu) != 0) ? 1u : 0u) | (__builtin_amdgcn_ballot_w64((item1 & 0xff00u) != 0) ? 2u : 0u) |
                      (__builtin_amdgcn_ballot_w64((item1 & 0xff0000u) != 0) ? 4u : 0u) | (__builtin_amdgcn_ballot_w64((item1 >> 24) != 0) ? 8u : 0u) |
                      (ecol >= 0 ? 1u << eb : 0u);
  const bool publish = ((pm >> wave) & 1u) != 0, row_on = ((pm >> myb) & 1u) != 0;
  const unsigned lse_off = g == 0 ? rb_row + RB_LSE : OOB;
  const unsigned tag_pub = ep_pub + 1u, tag_row = ep_row + 1u;
  if ((int)ctx4[0] <= 0 || (int)ctx4[1] <= 0 || (int)ctx4[2] <= 0 || (int)ctx4[3] <= 0) {
    const int l = myb == 0 ? ctx4[0] : myb == 1 ? ctx4[1] : myb == 2 ? ctx4[2] : ctx4[3];
#pragma unroll
    for (int s = 0; s < 4; ++s) qf[s] = l <= 0 ? u32x4{0, 0, 0, 0} : qf[s];
  }

  asm volatile("" :: "v"(pf));
  STAMP(7);

  const int phase = PLAG * 1024 / 10 - (STAG == 1 ? (int)wave * (PACE * 1024 / 80) : STAG == 2 ? (int)((split & 3) * 4 + wave) * (PACE * 1024 / 320) :
                                        STAG == 3 ? (int)(wave * 4 + (split & 3)) * (PACE * 1024 / 320) : STAG == 4 ? (int)((wave >> 1) * 8 + (split & 3) * 2 + (wave & 1)) * (PACE * 1024 / 320) : 0);
  int half = PACE * 1024 / 20, due = phase + half;
  const int due_last = phase + (2 * n - 1) * half;
  auto pace = [&]() {
    due += half;
    while ((int)(now() - tb0) < (due >> 10)) __builtin_amdgcn_s_sleep(1);
  };
  unsigned st = t_in;
  const unsigned st_off = lane < 8 ? WS_ST + 4 * lane : OOB;
  auto replan = [&](int i) {
    int d = (int)(st - t_in);
    d = (d > -400 && d < 400) ? d : 0;
    int sm = d + __builtin_amdgcn_mov_dpp(d, XOR1, 0xf, 0xf, true), mx = max(d, __builtin_amdgcn_mov_dpp(d, XOR1, 0xf, 0xf, true));
    sm += __builtin_amdgcn_mov_dpp(sm, XOR2, 0xf, 0xf, true); mx = max(mx, __builtin_amdgcn_mov_dpp(mx, XOR2, 0xf, 0xf, true));
    const int sa = __builtin_amdgcn_readlane(sm, 0), sb = __builtin_amdgcn_readlane(sm, 4);
    const int ma = max(__builtin_amdgcn_readlane(mx, 0), __builtin_amdgcn_readlane(mx, 4));
    const int rem = 2 * (n - 1 - i);
    const int shift = max(max((sa + sb) * 128, max(sa, sb) * 256 - n * (GAIN_IOD * 1024 / 10)), ma * 1024 - n * (GAIN_XCD * 1024 / 10));
    half = __builtin_amdgcn_readfirstlane((int)((float)(due_last + shift - due) * __builtin_amdgcn_rcpf((float)rem)));
  };
  auto hint = [&]() {
    const unsigned tk = __hip_atomic_fetch_add(ldsu + LDS_TK + lane, 1u, __ATOMIC_RELAXED, __HIP_MEMORY_SCOPE_WORKGROUP);
    asm volatile("" ::: "memory");
    const f32x4 ml0 = *reinterpret_cast<const f32x4*>(lds + LDS_ML + c * NWAVE * 2);
    const f32x4 ml1 = *reinterpret_cast<const f32x4*>(lds + LDS_ML + c * NWAVE * 2 + 4);
    const float M = fmaxf(fmaxf(ml0[0], ml0[2]), fmaxf(ml1[0], ml1[2]));
    const f32x4 f = f32x4{__builtin_amdgcn_exp2f(ml0[0] - M), __builtin_amdgcn_exp2f(ml0[2] - M), __builtin_amdgcn_exp2f(ml1[0] - M), __builtin_amdgcn_exp2f(ml1[2] - M)};
    const float L = f[0] * ml0[1] + f[1] * ml0[3] + f[2] * ml1[1] + f[3] * ml1[3];
    const bool last = tk == NWAVE - 2;
    __builtin_amdgcn_raw_buffer_store_b64(u32x2{__float_as_uint(row_on ? fmaxf(M + __builtin_amdgcn_logf(L), 0.5f * NEG_BIG) : NEG_BIG), tag_row}, wsr, last ? lse_off : OOB, split * LSTR, AUX_SC1);
    *reinterpret_cast<f32x4*>(lds + LDS_F + (last ? 0 : 16 * NWAVE) + c * NWAVE) = f * __builtin_amdgcn_rcpf(L);
    __builtin_amdgcn_sched_barrier(0);
  };
  auto step = [&](int i, auto more_) {
    constexpr bool MORE = decltype(more_)::value;
    const unsigned so1 = __builtin_amdgcn_readlane(item0, i + 1) * BLK_BYTES;
#pragma unroll
    for (int j = 0; j < 4; ++j) *reinterpret_cast<u32x4*>(ldsb + kw[j]) = kr[j];
    __builtin_amdgcn_sched_barrier(0);
    if (MORE) {
      if (SKEW && i >= 1 && i <= 3) replan(i); else if (SLK) __builtin_amdgcn_s_sleep(SLK);
      pace();
      if (SKEW) st = __builtin_amdgcn_raw_buffer_load_b32(wsr, st_off, 0, AUX_SC1);
#pragma unroll
      for (int j = 0; j < 4; ++j) kr[j] = __builtin_amdgcn_raw_buffer_load_b128(krs, GOFF(j), so1, KV_AUX);
      __builtin_amdgcn_sched_barrier(0);
    }
    const unsigned cn = __builtin_amdgcn_readlane(item1, i);
    f32x4 sc = f32x4{0.f, 0.f, 0.f, 0.f};
#pragma unroll
    for (int s = 0; s < 4; ++s) {
      const u32x4 kf = *reinterpret_cast<const u32x4*>(ldsb + kd[s]);
      sc = __builtin_amdgcn_mfma_f32_16x16x32_bf16(__builtin_bit_cast(bf16x8, kf), __builtin_bit_cast(bf16x8, qf[s]), sc, 0, 0, 0);
    }
    const int nv = (int)((cn >> csh) & 0xffu) - (int)g4;
    float x[4];
#pragma unroll
    for (int r = 0; r < 4; ++r) x[r] = r < nv ? sc[r] * C2 : -INFINITY;
    const float mn = fmaxf(m, xg_max(fmaxf(fmaxf(x[0], x[1]), fmaxf(x[2], x[3]))));
    const float alpha = __builtin_amdgcn_exp2f(m - mn);
    m = mn;
    float p[4];
#pragma unroll
    for (int r = 0; r < 4; ++r) p[r] = __builtin_amdgcn_exp2f(x[r] - mn);
    l = l * alpha + ((p[0] + p[1]) + (p[2] + p[3]));
    if (!MORE) {
      l = xg_sum(l);
      if (g == 0) *reinterpret_cast<f32x2*>(lds + LDS_ML + (c * NWAVE + wave) * 2) = f32x2{m, l};
      asm volatile("" ::: "memory");
      hint();
      STAMP(11);
    }
    u32x2 pk = u32x2{cvtpk(p[0], p[1]), cvtpk(p[2], p[3])};
#pragma unroll
    for (int t = 0; t < 8; ++t) { acc[t] *= alpha; asm volatile("" : "+v"(acc[t])); }
    asm volatile("" : "+v"(pk));
    __builtin_amdgcn_sched_barrier(0);
    u32x2 vt[8];
#pragma unroll
    for (int t = 0; t < 8; ++t) {
      const unsigned sel = (t & 1) ? 0x07060302u : 0x05040100u;
      vt[t] = u32x2{__builtin_amdgcn_perm(vr[1][t >> 1], vr[0][t >> 1], sel), __builtin_amdgcn_perm(vr[3][t >> 1], vr[2][t >> 1], sel)};
      if (MORE) asm volatile("" : "+v"(vt[t]));
    }
    if (MORE) {
      __builtin_amdgcn_sched_barrier(0);
      if (SLV) __builtin_amdgcn_s_sleep(SLV);
      pace();
#pragma unroll
      for (int j = 0; j < 4; ++j) vr[j] = __builtin_amdgcn_raw_buffer_load_b128(vrs, GOFF(j), so1, KV_AUX);
      __builtin_amdgcn_sched_barrier(0);
    }
#pragma unroll
    for (int t = 0; t < 8; ++t) {
      acc[t] = __builtin_amdgcn_mfma_f32_16x16x16bf16_1k(__builtin_bit_cast(s16x4, vt[t]), __builtin_bit_cast(s16x4, pk), acc[t], 0, 0, 0);
      if (!MORE) *reinterpret_cast<f32x4*>(ldsb + red + 64 * t) = acc[t];
    }
  };
  {
    int i = 0;
    do { step(i, std::true_type{}); } while (++i < n - 1);
    step(i, std::false_type{});
  }
  STAMP(2);

  asm volatile("s_waitcnt lgkmcnt(0)\n\ts_barrier" ::: "memory");
  STAMP(3);
  if (publish) {
    const unsigned r = 4 * wave + g;
    const f32x4 f = *reinterpret_cast<const f32x4*>(lds + LDS_F + r * NWAVE);
    f32x4 a[NWAVE][2];
#pragma unroll
    for (int w = 0; w < NWAVE; ++w) {
      a[w][0] = *reinterpret_cast<const f32x4*>(lds + LDS_RED + (w * 16 + r) * RED_ROW + 32 * (c >> 2) + 4 * (c & 3));
      a[w][1] = *reinterpret_cast<const f32x4*>(lds + LDS_RED + (w * 16 + r) * RED_ROW + 32 * (c >> 2) + 4 * (c & 3) + 16);
    }
    __builtin_amdgcn_sched_barrier(0);
    const f32x4 oe = f[0] * a[0][0] + f[1] * a[1][0] + f[2] * a[2][0] + f[3] * a[3][0];
    const f32x4 oo = f[0] * a[0][1] + f[1] * a[1][1] + f[2] * a[2][1] + f[3] * a[3][1];
    const unsigned po = rb_pub + RB_PO + c * 16;
#pragma unroll
    for (int i = 0; i < 2; ++i)
      __builtin_amdgcn_raw_buffer_store_b128(u32x4{cvtpk(oe[2 * i], oo[2 * i]), tag_pub, cvtpk(oe[2 * i + 1], oo[2 * i + 1]), tag_pub}, wsr, po + i * 256, split * 512, AUX_SC1);
  }
  STAMP(4);
  STAMP_FLUSH();
}

}

extern "C" void kernel_launch(void* const* d_in, const int* in_sizes, int n_in, void* d_out, int out_size, void* d_ws, size_t ws_size,
                              hipStream_t stream) {
  attn_decode<<<HKV * (NSPLIT + 16), NTHR, 0, stream>>>((const int*)d_in[3], (const int*)d_in[4], (const unsigned short*)d_in[0], (unsigned char*)d_ws,
                                                        (const unsigned short*)d_in[1], (const unsigned short*)d_in[2], (unsigned short*)d_out,
                                                        (unsigned long long)in_sizes[1] * 2ull);
}
```

```cpp
#include <hip/hip_runtime.h>
#include <hip/hip_bf16.h>
#include <cstdint>
#include <type_traits>

namespace {

constexpr int B = 4, H = 32, HKV = 8, D = 128, BS = 16, NB = 1024;
constexpr unsigned LK = NB * BS;
constexpr int NSPLIT = 32;
constexpr int NWAVE = 4, NTHR = NWAVE * 64;
constexpr unsigned NG = NB / NWAVE;
constexpr unsigned NEARLY = NSPLIT / B;
constexpr unsigned WIDE = 8;
constexpr int PACE = 1066;
constexpr int PLAG = 410;
constexpr int COLD_AT = 700;
constexpr int COLD_BACK = 510;
constexpr unsigned ROUNDS = 0x1f8u;
constexpr int LATE_NS = 250;
constexpr int LATE_COLD = 180;
constexpr int LATE_MIN = 9;
constexpr int SLOW_BY = 91;
constexpr int PAUSEK = 3;
constexpr unsigned SPIN_CAP = 1u << 18;
constexpr float C2 = 0.088388347648318440f * 1.4426950408889634f;
constexpr int BLK_BYTES = BS * HKV * D * 2;
constexpr float NEG_BIG = -3.0e38f;
constexpr int AUX_NT = 2, AUX_SC1 = 16;
constexpr unsigned OOB = 0x80000000u;

using u32x4 = __attribute__((ext_vector_type(4))) unsigned;
using u32x2 = __attribute__((ext_vector_type(2))) unsigned;
using u64x2 = __attribute__((ext_vector_type(2))) unsigned long long;
using f32x4 = __attribute__((ext_vector_type(4))) float;
using f32x2 = __attribute__((ext_vector_type(2))) float;
using bf16x8 = __attribute__((ext_vector_type(8))) __bf16;
using bf16x2 = __attribute__((ext_vector_type(2))) __bf16;
using s16x4 = __attribute__((ext_vector_type(4))) short;
typedef __attribute__((address_space(4))) const unsigned cu32;
typedef __attribute__((address_space(4))) const u32x4 cu32x4;
typedef __attribute__((address_space(1))) const unsigned gu32;

constexpr unsigned RB_LSE = 256, RB_PO = RB_LSE + NSPLIT * 8, RB_BYTES = RB_PO + NSPLIT * 512;
constexpr unsigned WS_LATE = B * H * RB_BYTES;
constexpr unsigned WS_USED = WS_LATE + 4 * 512;

constexpr int RED_ROW = D + 4;
constexpr int LDS_LCP = 0;
constexpr int LDS_TK = 64;
constexpr int LDS_ML = 128;
constexpr int LDS_F = LDS_ML + NWAVE * 16 * 2;
constexpr int LDS_K = LDS_F + 2 * 16 * NWAVE;
constexpr int LDS_Q = LDS_K + NWAVE * 1024;
constexpr int LDS_TAB = LDS_Q + 1024;
constexpr int LDS_RED = LDS_TAB + B * NB;
constexpr int LDS_WORDS = LDS_RED + NWAVE * 16 * RED_ROW;

__device__ __forceinline__ unsigned cvtpk(float lo, float hi) {
  return __builtin_bit_cast(unsigned, __builtin_convertvector(f32x2{lo, hi}, bf16x2));
}
__device__ __forceinline__ float xg_max(float x) {
  auto s = __builtin_amdgcn_permlane16_swap(__float_as_uint(x), __float_as_uint(x), false, false);
  x = fmaxf(__uint_as_float(s[0]), __uint_as_float(s[1]));
  auto t = __builtin_amdgcn_permlane32_swap(__float_as_uint(x), __float_as_uint(x), false, false);
  return fmaxf(__uint_as_float(t[0]), __uint_as_float(t[1]));
}
__device__ __forceinline__ float xg_sum(float x) {
  auto s = __builtin_amdgcn_permlane16_swap(__float_as_uint(x), __float_as_uint(x), false, false);
  x = __uint_as_float(s[0]) + __uint_as_float(s[1]);
  auto t = __builtin_amdgcn_permlane32_swap(__float_as_uint(x), __float_as_uint(x), false, false);
  return __uint_as_float(t[0]) + __uint_as_float(t[1]);
}
template <int C> __device__ __forceinline__ float dppf(float x) {
  return __builtin_bit_cast(float, __builtin_amdgcn_mov_dpp(__builtin_bit_cast(int, x), C, 0xf, 0xf, true));
}
constexpr int XOR1 = 0xB1, XOR2 = 0x4E, ROR4 = 0x124, BCAST = 0x150;
template <int LANE>
__device__ __forceinline__ void first_diff(unsigned& cand, u32x4 a, u32x4 b) {
  const u64x2 x = __builtin_bit_cast(u64x2, a), y = __builtin_bit_cast(u64x2, b);
  const unsigned long long mk = __builtin_amdgcn_ballot_w64(x[0] != y[0]) | __builtin_amdgcn_ballot_w64(x[1] != y[1]);
  unsigned r; asm("s_ff1_i32_b64 %1, %2\n\tv_writelane_b32 %0, %1, %3" : "+v"(cand), "=&s"(r) : "s"(mk), "i"(LANE));
}
__device__ __forceinline__ unsigned seq_len(unsigned c) { return min(c - 1u, LK - 1u) + 1u; }
__device__ __forceinline__ unsigned now() { return (unsigned)__builtin_amdgcn_s_memrealtime(); }
#define FENCE() __builtin_amdgcn_sched_barrier(0)

__global__ void __attribute__((aligned(4096))) __launch_bounds__(NTHR, 2)
attn_decode(const int* __restrict__ bt, const int* __restrict__ ctx, const unsigned short* __restrict__ q, unsigned char* ws,
            const unsigned short* __restrict__ kc, const unsigned short* __restrict__ vc, unsigned short* __restrict__ out, unsigned long long kv_bytes64) {
  __shared__ __attribute__((aligned(16))) float lds[LDS_WORDS];
  unsigned* ldsu = reinterpret_cast<unsigned*>(lds);
  char* ldsb = reinterpret_cast<char*>(lds);
  const unsigned tid = threadIdx.x, lane = tid & 63, wave = __builtin_amdgcn_readfirstlane(tid >> 6);
  const unsigned g = lane >> 4, c = lane & 15;
  const unsigned split = blockIdx.x >> 3;
  const unsigned h = (blockIdx.x + (blockIdx.x >> 8)) & 7;
  unsigned goff = g * 8192 + h * 256 + 16 * c;
  unsigned goff2 = goff + 4096;
  unsigned touch = (split - NSPLIT + 16 * wave) * ((1u << 24) + 37120);
  touch = split < NSPLIT ? 0xffffffffu : touch;
  asm volatile("" : "+s"(touch));
  ldsu[LDS_LCP + lane] = 0xffffffffu; ldsu[LDS_TK + lane] = 0xffffffffu;
  asm volatile("s_waitcnt lgkmcnt(0)" : "+v"(goff), "+v"(goff2) : "s"(bt), "s"(ctx), "s"(q), "s"(ws), "s"(kc), "s"(vc), "s"(out), "s"(kv_bytes64) : "memory");
  {
    unsigned a, b;
    asm volatile("s_cmp_ge_u32 %4, %5\n\ts_cbranch_scc1 1f\n\ts_load_dword %0, %2, %4 offset:0\n\ts_load_dword %1, %3, %4 offset:0\n\ts_waitcnt lgkmcnt(0)\n1:"
                 : "=&s"(a), "=&s"(b) : "s"(kc), "s"(vc), "s"(touch), "s"((unsigned)kv_bytes64) : "scc");
  }
  const __amdgpu_buffer_rsrc_t wsr = __builtin_amdgcn_make_buffer_rsrc((void*)ws, 0, WS_USED, 0x00020000);

  if (__builtin_expect(split >= NSPLIT, 0)) {
    const __amdgpu_buffer_rsrc_t outr = __builtin_amdgcn_make_buffer_rsrc((void*)out, 0, B * H * D * 2, 0x00020000);
    const unsigned crow = split - NSPLIT, rb_own = (h * 16 + crow) * RB_BYTES;
    if (crow == 0 && wave == 1) {
      unsigned long long pc; asm volatile("s_getpc_b64 %0" : "=s"(pc));
      const unsigned cpf = ((gu32*)((pc & ~4095ull) - 4096))[32 * lane];
      asm volatile("" :: "v"(cpf));
    }
    const unsigned tag = __builtin_amdgcn_raw_buffer_load_b32(wsr, 0, rb_own, AUX_SC1) + 1u;
    const unsigned loff = rb_own + RB_LSE + (4 * (c & 7) + g) * 8;
    unsigned ooff = g == 0 ? 2 * (32 * (c & 3) + 8 * wave + 2 * (c >> 2)) : OOB;
    unsigned dbase = rb_own + RB_PO + g * 512 + wave * 128 + c * 8;
    asm volatile("s_barrier" : "+v"(ooff), "+v"(dbase) : "v"(tag) : "memory");
    unsigned obase = ((crow >> 2) * H + 4 * h + (crow & 3)) * D * 2;
    asm volatile("" : "+s"(obase));
    unsigned db[4];
#pragma unroll
    for (int s = 0; s < 4; ++s) { db[s] = dbase + s * 4096; asm volatile("" : "+v"(db[s])); }
    u32x2 lg, x[8];
    unsigned spins = 0;
    if (wave != 0) {
      const unsigned j2 = crow + 16 * (wave - 1), pg = j2 * ((1u << 21) + 4352) + (1u << 20);
      for (unsigned z = 0; z < 3 + (j2 & 7); ++z) __builtin_amdgcn_s_sleep(8);
      if (pg < (unsigned)kv_bytes64) {
        unsigned a, b;
        asm volatile("s_load_dword %0, %2, %4 offset:0\n\ts_load_dword %1, %3, %4 offset:0\n\ts_waitcnt lgkmcnt(0)" : "=&s"(a), "=&s"(b) : "s"(kc), "s"(vc), "s"(pg));
      }
    }
    if (wave != 0) __builtin_amdgcn_s_barrier();
    bool seen = wave != 0;
#pragma unroll 1
    for (;;) {
      asm volatile("buffer_load_dwordx2 %0, %1, %2, 0 offen sc1\n\ts_waitcnt vmcnt(0)" : "=&v"(lg) : "v"(loff), "s"(wsr) : "memory");
      const unsigned long long miss = __builtin_amdgcn_ballot_w64(lg[1] != tag);
      ++spins;
      if (!seen && (__builtin_popcountll(miss) <= 56 || spins > SPIN_CAP)) { seen = true; __builtin_amdgcn_s_barrier(); }
      if (miss == 0 || spins > SPIN_CAP) break;
      if (miss == ~0ull) __builtin_amdgcn_s_sleep(12); else __builtin_amdgcn_s_sleep(1);
    }
    __builtin_amdgcn_s_sleep(2);
    const float lse = __uint_as_float(lg[0]);
    const int absent = lse == NEG_BIG ? (int)OOB : 0;
    unsigned doff[8];
#define DOFF(S) doff[S] = (unsigned)__builtin_amdgcn_mov_dpp(absent, BCAST + S, 0xf, 0xf, true) | db[S >> 1];
    DOFF(0) DOFF(1) DOFF(2) DOFF(3) DOFF(4) DOFF(5) DOFF(6) DOFF(7)
#undef DOFF
#define LOAD_ROWS() do { asm volatile("" ::: "memory"); _Pragma("unroll") for (int s = 0; s < 8; ++s) \
      x[s] = __builtin_amdgcn_raw_buffer_load_b64(wsr, doff[s] + 2048 * (s & 1), 0, AUX_SC1); \
      FENCE(); __builtin_amdgcn_raw_buffer_store_b32(tag, wsr, tid == 0 ? 0u : OOB, rb_own, AUX_SC1); asm volatile("" ::: "memory"); } while (0)
    LOAD_ROWS();
    FENCE();
    float w[8]; unsigned want[8];
    {
      float M = fmaxf(lse, dppf<XOR1>(lse)); M = fmaxf(M, dppf<XOR2>(M)); M = fmaxf(M, dppf<ROR4>(M)); M = xg_max(M);
      const float wk = __builtin_amdgcn_exp2f(lse - M);
      float L = wk + dppf<XOR1>(wk); L += dppf<XOR2>(L); L += dppf<ROR4>(L);
      const float wn = wk * __builtin_amdgcn_rcpf(xg_sum(L));
      w[0] = dppf<BCAST + 0>(wn); w[1] = dppf<BCAST + 1>(wn); w[2] = dppf<BCAST + 2>(wn); w[3] = dppf<BCAST + 3>(wn);
      w[4] = dppf<BCAST + 4>(wn); w[5] = dppf<BCAST + 5>(wn); w[6] = dppf<BCAST + 6>(wn); w[7] = dppf<BCAST + 7>(wn);
#pragma unroll
      for (int s = 0; s < 8; ++s) { want[s] = (int)doff[s] < 0 ? 0u : tag; asm volatile("" : "+v"(w[s]), "+v"(want[s])); }
    }
    float o0, o1;
#pragma unroll 1
    for (;;) {
      unsigned bad = 0;
      o0 = 0.f; o1 = 0.f;
#pragma unroll
      for (int s = 0; s < 8; ++s) {
        bad = __builtin_amdgcn_bitop3_b32(x[s][1], want[s], bad, 0xBE);
        o0 = __builtin_fmaf(w[s], __uint_as_float(x[s][0] << 16), o0); o1 = __builtin_fmaf(w[s], __uint_as_float(x[s][0] & 0xffff0000u), o1);
      }
      asm volatile("" : "+v"(o0), "+v"(o1));
      if (__builtin_amdgcn_ballot_w64(bad != 0) == 0) break;
      if (++spins > SPIN_CAP) break;
      LOAD_ROWS();
    }
#undef LOAD_ROWS
    o0 = xg_sum(o0); o1 = xg_sum(o1);
    __builtin_amdgcn_raw_buffer_store_b32(cvtpk(o0, o1), outr, ooff, obase, 0);
    return;
  }

  const unsigned eb = split >> 3;
  unsigned ek = (split & 7) * NWAVE + 3 - wave, erow = eb * NB * 4, kvb = (unsigned)kv_bytes64;
  unsigned ectx = ((cu32*)ctx)[eb];
  u32x4 ctx4 = *(cu32x4*)ctx;
  const __amdgpu_buffer_rsrc_t btr = __builtin_amdgcn_make_buffer_rsrc((void*)bt, 0, B * NB * 4, 0x00020000);
  u32x4 tb[B];
#pragma unroll
  for (int b = 0; b < B; ++b) tb[b] = __builtin_amdgcn_raw_buffer_load_b128(btr, 16 * tid, b * NB * 4, 0);
  FENCE();
  const __amdgpu_buffer_rsrc_t qr = __builtin_amdgcn_make_buffer_rsrc((void*)q, 0, B * H * D * 2, 0x00020000);
  u32x4 qt = __builtin_amdgcn_raw_buffer_load_b128(qr, g * D * 2 + 16 * c, (wave * H + 4 * h) * D * 2, 0);
  const unsigned myb = c >> 2;
  const unsigned rb_pub = (h * 16 + 4 * wave + g) * RB_BYTES, rb_row = (h * 16 + c) * RB_BYTES;
  float m = NEG_BIG, l = 0.f;
  f32x4 acc[8];
#pragma unroll
  for (int t = 0; t < 8; ++t) { acc[t] = f32x4{0.f, 0.f, 0.f, 0.f}; asm volatile("" : "+v"(acc[t])); }
  unsigned csh = 8 * myb, g4 = 4 * g;
  unsigned kw[4], kd[4];
#pragma unroll
  for (int j = 0; j < 4; ++j) {
    kw[j] = (LDS_K + wave * 1024) * 4 + ((4 * g + j) * 16 + (c ^ (4 * g + j))) * 16;
    kd[j] = (LDS_K + wave * 1024) * 4 + (c * 16 + ((4 * j + g) ^ c)) * 16;
    asm volatile("" : "+v"(kw[j]), "+v"(kd[j]));
  }
  unsigned red = (LDS_RED + (wave * 16 + c) * RED_ROW + 4 * g) * 4;
  asm volatile("" : "+v"(csh), "+v"(g4), "+v"(red), "+s"(ek), "+s"(erow), "+s"(kvb));
  FENCE();
  asm volatile("" : "+s"(ectx));
  const unsigned elen = seq_len(ectx);
  const int elast = (int)((elen - 1u) / BS), ecol = (elast | 3) - (int)ek;
  unsigned ephys;
  asm volatile("s_load_dword %0, %1, %2 offset:0\n\ts_waitcnt lgkmcnt(0)" : "=&s"(ephys) : "s"(bt), "s"(erow + 4 * min(max(ecol, 0), elast)));
  unsigned tb0 = now();
  FENCE();
#define GOFF(j) (((j) & 2 ? goff2 : goff) + 2048 * ((j) & 1))
  u32x4 kr[4], vr[4];
  const __amdgpu_buffer_rsrc_t krs = __builtin_amdgcn_make_buffer_rsrc((void*)kc, 0, kvb, 0x00020000);
  const __amdgpu_buffer_rsrc_t vrs = __builtin_amdgcn_make_buffer_rsrc((void*)vc, 0, kvb, 0x00020000);
#pragma unroll
  for (int j = 0; j < 4; ++j) kr[j] = __builtin_amdgcn_raw_buffer_load_b128(krs, GOFF(j), ephys * BLK_BYTES, AUX_NT);
  FENCE();
  const unsigned ep_pub = __builtin_amdgcn_raw_buffer_load_b32(wsr, rb_pub, 0, AUX_SC1);
  const unsigned ep_row = __builtin_amdgcn_raw_buffer_load_b32(wsr, rb_row, 0, AUX_SC1);
  FENCE();
  asm volatile("" : "+s"(ctx4));
  unsigned nc0 = max((seq_len(ctx4[0]) + 4 * BS - 1) / (4 * BS), NEARLY) - NEARLY, nc1 = max((seq_len(ctx4[1]) + 4 * BS - 1) / (4 * BS), NEARLY) - NEARLY;
  unsigned nc2 = max((seq_len(ctx4[2]) + 4 * BS - 1) / (4 * BS), NEARLY) - NEARLY, nc3 = max((seq_len(ctx4[3]) + 4 * BS - 1) / (4 * BS), NEARLY) - NEARLY;
  unsigned e1 = (ecol >= 0 && ecol <= elast) ? min(elen - BS * ecol, (unsigned)BS) << (8 * eb) : 0u;
  asm volatile("" : "+v"(nc0), "+v"(nc1), "+v"(nc2), "+v"(nc3), "+s"(e1));
  FENCE();

  asm volatile("s_barrier" ::: "memory");
#pragma unroll
  for (int b = 0; b < B; ++b) *reinterpret_cast<u32x4*>(ldsu + LDS_TAB + b * NB + 4 * tid) = tb[b];
  {
    unsigned cand = 0xffffffffu;
    first_diff<0>(cand, tb[1], tb[0]);
    first_diff<1>(cand, tb[2], tb[0]);
    first_diff<2>(cand, tb[3], tb[0]);
    first_diff<3>(cand, tb[2], tb[1]);
    first_diff<4>(cand, tb[3], tb[1]);
    first_diff<5>(cand, tb[3], tb[2]);
    __hip_atomic_fetch_min(ldsu + LDS_LCP + lane, cand | (wave << 6), __ATOMIC_RELAXED, __HIP_MEMORY_SCOPE_WORKGROUP);
    const int cw = wave == 0 ? ctx4[0] : wave == 1 ? ctx4[1] : wave == 2 ? ctx4[2] : ctx4[3];
    if (cw <= 0) qt = u32x4{0, 0, 0, 0};
    *reinterpret_cast<u32x4*>(ldsu + LDS_Q + ((4 * wave + g) * 16 + (c ^ (4 * wave + g))) * 4) = qt;
  }
  asm volatile("s_waitcnt lgkmcnt(0)\n\ts_barrier" ::: "memory");
  const unsigned tv0 = now();
  FENCE();
#pragma unroll
  for (int j = 0; j < 4; ++j) vr[j] = __builtin_amdgcn_raw_buffer_load_b128(vrs, GOFF(j), ephys * BLK_BYTES, AUX_NT);
  FENCE();
  unsigned reports = __builtin_amdgcn_raw_buffer_load_b32(wsr, OOB + lane, 0, 0);
  FENCE();
  const u32x4 la = *reinterpret_cast<const u32x4*>(ldsu + LDS_LCP);
  const u32x2 lb = *reinterpret_cast<const u32x2*>(ldsu + LDS_LCP + 4);
  const unsigned l10 = min(la[0], min(nc0, nc1)), l20 = min(la[1], min(nc0, nc2)), l30 = min(la[2], min(nc0, nc3));
  const unsigned l21 = min(la[3], min(nc1, nc2)), l31 = min(lb[0], min(nc1, nc3)), l32 = min(lb[1], min(nc2, nc3));
  const unsigned P1 = l10, P2 = max(l20, l21), P3 = max(l30, max(l31, l32));
  const unsigned B1 = nc0, B2 = B1 + nc1 - P1, B3 = B2 + nc2 - P2, total = B3 + nc3 - P3;
  const unsigned su = split / WIDE;
  const unsigned lo = (total * su) / (NSPLIT / WIDE) + split % WIDE, nm = ((total * su + total) / (NSPLIT / WIDE) + WIDE - 1 - lo) / WIDE;
  const unsigned pos = lo + WIDE * (lane - 1u);
  unsigned idx = pos;
  idx = pos >= B1 ? pos + (NG + P1 - B1) : idx;
  idx = pos >= B2 ? pos + (2 * NG + P2 - B2) : idx;
  idx = pos >= B3 ? pos + (3 * NG + P3 - B3) : idx;
  idx &= B * NG - 1;
  const bool live = lane - 1u < nm;
  unsigned item0 = ldsu[LDS_TAB + 4 * idx + wave];
  item0 = live ? item0 : 0u;
  const int n = max(__builtin_amdgcn_readfirstlane(nm), 1) + 1;
  unsigned item1;
  {
    const unsigned ib = idx / NG, j = idx % NG;
    const unsigned x1 = ib == 0 ? l10 : 0u, x2 = ib == 0 ? l20 : ib == 1 ? l21 : 0u, x3 = ib == 0 ? l30 : ib == 1 ? l31 : l32;
    item1 = (16u << (8 * ib)) | (j < x1 ? 16u << 8 : 0u) | (j < x2 ? 16u << 16 : 0u) | (j < x3 ? 16u << 24 : 0u);
    item1 = live ? item1 : 0u;
    item1 = lane == 0 ? e1 : item1;
  }
  const unsigned pm = (__builtin_amdgcn_ballot_w64((item1 & 0xffu) != 0) ? 1u : 0u) | (__builtin_amdgcn_ballot_w64((item1 & 0xff00u) != 0) ? 2u : 0u) |
                      (__builtin_amdgcn_ballot_w64((item1 & 0xff0000u) != 0) ? 4u : 0u) | (__builtin_amdgcn_ballot_w64((item1 >> 24) != 0) ? 8u : 0u) |
                      (ecol >= 0 ? 1u << eb : 0u);
  const bool publish = ((pm >> wave) & 1u) != 0, row_on = ((pm >> myb) & 1u) != 0;
  u32x4 qf[4];
#pragma unroll
  for (int s = 0; s < 4; ++s) qf[s] = *reinterpret_cast<const u32x4*>(ldsb + kd[s] + (NWAVE - wave) * 4096);

  const bool cold_start = tv0 - tb0 > COLD_AT / 10;
  if (cold_start) tb0 = tv0 - COLD_BACK / 10;
  constexpr int half0 = PACE * 1024 / 20;
  int half = half0, due = PLAG * 1024 / 10 - (int)(NSPLIT - 1 - split) * (half0 / NSPLIT) + half0, slack = 0, slack2 = 0;
  unsigned rounds = ROUNDS;
  const int late_at = cold_start ? LATE_COLD / 10 : LATE_NS / 10;
  auto pace = [&](bool start) {
    due += half;
    int t = (int)(now() - tb0);
    if (start) due = max(due, t << 10);
    slack2 = slack + ((due >> 10) - t); slack = (due >> 10) - t;
    while (t < (due >> 10)) { __builtin_amdgcn_s_sleep(1); t = (int)(now() - tb0); }
  };
  const unsigned rep_off = (tid == 0 && (split & 15) == 8 * (h & 1) + 3) ? WS_LATE + ((split >> 4) * 8 + h) * 8 : OOB;
  const unsigned rd_off = lane < 16 ? WS_LATE + 8 * lane : OOB;

  auto step = [&](int i, auto more_) {
    constexpr bool MORE = decltype(more_)::value;
    const unsigned so1 = __builtin_amdgcn_readlane(item0, i + 1) * BLK_BYTES;
#pragma unroll
    for (int j = 0; j < 4; ++j) *reinterpret_cast<u32x4*>(ldsb + kw[j]) = kr[j];
    FENCE();
    if (MORE) {
      pace(i == 0);
#pragma unroll
      for (int j = 0; j < 4; ++j) kr[j] = __builtin_amdgcn_raw_buffer_load_b128(krs, GOFF(j), so1, AUX_NT);
      FENCE();
      __builtin_amdgcn_raw_buffer_store_b32(tb0 + (due >> 10), wsr, (((rounds >> i) & 1) && slack2 < late_at) ? rep_off : OOB, (i & 3) * 512, AUX_SC1);
      FENCE();
    }
    const unsigned cn = __builtin_amdgcn_readlane(item1, i);
    f32x4 sc = f32x4{0.f, 0.f, 0.f, 0.f};
#pragma unroll
    for (int s = 0; s < 4; ++s) {
      const u32x4 kf = *reinterpret_cast<const u32x4*>(ldsb + kd[s]);
      sc = __builtin_amdgcn_mfma_f32_16x16x32_bf16(__builtin_bit_cast(bf16x8, kf), __builtin_bit_cast(bf16x8, qf[s]), sc, 0, 0, 0);
    }
    const int nv = (int)((cn >> csh) & 0xffu) - (int)g4;
    float x[4];
#pragma unroll
    for (int r = 0; r < 4; ++r) x[r] = r < nv ? sc[r] * C2 : -INFINITY;
    const float mn = fmaxf(m, xg_max(fmaxf(fmaxf(x[0], x[1]), fmaxf(x[2], x[3]))));
    const float alpha = __builtin_amdgcn_exp2f(m - mn);
    m = mn;
    float p[4];
#pragma unroll
    for (int r = 0; r < 4; ++r) p[r] = __builtin_amdgcn_exp2f(x[r] - mn);
    l = l * alpha + ((p[0] + p[1]) + (p[2] + p[3]));
    if (!MORE) {
      l = xg_sum(l);
      if (g == 0) *reinterpret_cast<f32x2*>(lds + LDS_ML + (c * NWAVE + wave) * 2) = f32x2{m, l};
      asm volatile("" :: "v"(ep_pub), "v"(ep_row) : "memory");
      const unsigned tk = __hip_atomic_fetch_add(ldsu + LDS_TK + lane, 1u, __ATOMIC_RELAXED, __HIP_MEMORY_SCOPE_WORKGROUP);
      asm volatile("" ::: "memory");
      const f32x4 ml0 = *reinterpret_cast<const f32x4*>(lds + LDS_ML + c * NWAVE * 2);
      const f32x4 ml1 = *reinterpret_cast<const f32x4*>(lds + LDS_ML + c * NWAVE * 2 + 4);
      const float M = fmaxf(fmaxf(ml0[0], ml0[2]), fmaxf(ml1[0], ml1[2]));
      const f32x4 f = f32x4{__builtin_amdgcn_exp2f(ml0[0] - M), __builtin_amdgcn_exp2f(ml0[2] - M), __builtin_amdgcn_exp2f(ml1[0] - M), __builtin_amdgcn_exp2f(ml1[2] - M)};
      const float L = f[0] * ml0[1] + f[1] * ml0[3] + f[2] * ml1[1] + f[3] * ml1[3];
      const bool last = tk == NWAVE - 2;
      __builtin_amdgcn_raw_buffer_store_b64(u32x2{__float_as_uint(row_on ? fmaxf(M + __builtin_amdgcn_logf(L), 0.5f * NEG_BIG) : NEG_BIG), ep_row + 1u},
                                            wsr, (last && g == 0) ? rb_row + RB_LSE : OOB, split * 8, AUX_SC1);
      *reinterpret_cast<f32x4*>(lds + LDS_F + (last ? 0 : 16 * NWAVE) + c * NWAVE) = f * __builtin_amdgcn_rcpf(L);
      FENCE();
    }
    u32x2 pk = u32x2{cvtpk(p[0], p[1]), cvtpk(p[2], p[3])};
#pragma unroll
    for (int t = 0; t < 8; ++t) { acc[t] *= alpha; asm volatile("" : "+v"(acc[t])); }
    asm volatile("" : "+v"(pk));
    FENCE();
#pragma unroll
    for (int t = 0; t < 8; ++t) {
      const unsigned sel = (t & 1) ? 0x07060302u : 0x05040100u;
      const u32x2 vt = u32x2{__builtin_amdgcn_perm(vr[1][t >> 1], vr[0][t >> 1], sel), __builtin_amdgcn_perm(vr[3][t >> 1], vr[2][t >> 1], sel)};
      acc[t] = __builtin_amdgcn_mfma_f32_16x16x16bf16_1k(__builtin_bit_cast(s16x4, vt), __builtin_bit_cast(s16x4, pk), acc[t], 0, 0, 0);
      if (MORE) asm volatile("" : "+v"(acc[t])); else *reinterpret_cast<f32x4*>(ldsb + red + 64 * t) = acc[t];
    }
    if (MORE) {
      FENCE();
      if ((rounds << 2 >> i) & 1) {
        const int late = __builtin_popcountll(__builtin_amdgcn_ballot_w64(tb0 + (due >> 10) - reports < 400u));
        if (late >= LATE_MIN) { half += SLOW_BY * 1024 / 20; due += (2 * i + PAUSEK) * (SLOW_BY * 1024 / 20); rounds = 0; }
      }
      pace(i == 0);
#pragma unroll
      for (int j = 0; j < 4; ++j) vr[j] = __builtin_amdgcn_raw_buffer_load_b128(vrs, GOFF(j), so1, AUX_NT);
      FENCE();
      reports = __builtin_amdgcn_raw_buffer_load_b32(wsr, ((rounds << 1 >> i) & 1) ? rd_off : OOB, ((i - 1) & 3) * 512, AUX_SC1);
      FENCE();
    }
  };
  {
    int i = 0;
    do { step(i, std::true_type{}); } while (++i < n - 1);
    step(i, std::false_type{});
  }

  asm volatile("s_waitcnt lgkmcnt(0)\n\ts_barrier" ::: "memory");
  if (publish) {
    const unsigned r = 4 * wave + g, tag = ep_pub + 1u;
    const f32x4 f = *reinterpret_cast<const f32x4*>(lds + LDS_F + r * NWAVE);
    f32x4 a[NWAVE][2];
#pragma unroll
    for (int w = 0; w < NWAVE; ++w) {
      a[w][0] = *reinterpret_cast<const f32x4*>(lds + LDS_RED + (w * 16 + r) * RED_ROW + 32 * (c >> 2) + 4 * (c & 3));
      a[w][1] = *reinterpret_cast<const f32x4*>(lds + LDS_RED + (w * 16 + r) * RED_ROW + 32 * (c >> 2) + 4 * (c & 3) + 16);
    }
    FENCE();
    const f32x4 oe = f[0] * a[0][0] + f[1] * a[1][0] + f[2] * a[2][0] + f[3] * a[3][0];
    const f32x4 oo = f[0] * a[0][1] + f[1] * a[1][1] + f[2] * a[2][1] + f[3] * a[3][1];
#pragma unroll
    for (int k = 0; k < 4; ++k)
      __builtin_amdgcn_raw_buffer_store_b64(u32x2{cvtpk(oe[k], oo[k]), tag}, wsr, rb_pub + RB_PO + c * 8 + k * 128, split * 512, AUX_SC1);
  }
}

}

extern "C" void kernel_launch(void* const* d_in, const int* in_sizes, int n_in, void* d_out, int out_size, void* d_ws, size_t ws_size,
                              hipStream_t stream) {
  attn_decode<<<HKV * (NSPLIT + 16), NTHR, 0, stream>>>((const int*)d_in[3], (const int*)d_in[4], (const unsigned short*)d_in[0], (unsigned char*)d_ws,
                                                        (const unsigned short*)d_in[1], (const unsigned short*)d_in[2], (unsigned short*)d_out,
                                                        (unsigned long long)in_sizes[1] * 2ull);
}
```
